```python
import torch

def _softmax(x, axis=-1):
    return torch.softmax(x, dim=axis)
def _gelu(x):
    return torch.nn.functional.gelu(x, approximate="tanh")
def _silu(x):
    return torch.nn.functional.silu(x)
def _sigmoid(x):
    return torch.sigmoid(x)
def _layernorm(x, axis=-1, eps=1e-5):
    m = x.mean(dim=axis, keepdim=True)
    v = x.var(dim=axis, keepdim=True, unbiased=False)
    return (x - m) / torch.sqrt(v + eps)
def _scatter_add(tgt, idx, src, axis=0):
    return tgt.clone().index_add_(axis, idx, src)
def _segment_sum(src, seg, n):
    out = torch.zeros((n,) + src.shape[1:], dtype=src.dtype)
    return out.index_add_(0, seg, src)
def _pad(x, pw):
    flat = tuple(v for pair in reversed(pw) for v in pair)
    return torch.nn.functional.pad(x, flat)

kernel_name = 'standalone__controlnet__diffusion'

def setup_inputs():

    _w = (lambda a: (lambda t: t.to(torch.bfloat16) if t.is_floating_point() else t)(torch.as_tensor(a)))
    B, HW, C = 2, 128, 128
    sc = C ** -0.5
    return {
        "x_frozen": _w((lambda *s: torch.randn(*s, dtype=torch.bfloat16) * 0.05)(B, HW, HW, C)),
        "x": _w((lambda *s: torch.randn(*s, dtype=torch.bfloat16) * 0.1)(B, HW, HW, C)),
        "c": _w((lambda *s: torch.randn(*s, dtype=torch.bfloat16) * 0.4)(B, HW, HW, C)),
        "w_z1": _w((lambda *s: torch.randn(*s, dtype=torch.bfloat16) * 0.4)(C, C) * sc),
        "w_z2": _w((lambda *s: torch.randn(*s, dtype=torch.bfloat16) * 0.1)(C, C) * sc),
        "w_conv": _w((lambda *s: torch.randn(*s, dtype=torch.bfloat16) * 0.1)(3, 3, C, C) * sc / 3.0),
    }

def reference(x_frozen, x, c, w_z1, w_z2, w_conv):
    B, H, W, C = x.shape
    h_in = x + torch.einsum("bhwc,cd->bhwd", c, w_z1)
    h = _silu(_layernorm(h_in, axis=-1))
    hp = _pad(h, ((0, 0), (1, 1), (1, 1), (0, 0)))
    acc = h * 0.0
    for di in range(3):
        for dj in range(3):
            acc = acc + torch.einsum("bhwc,cd->bhwd", hp[:, di:di + H, dj:dj + W, :], w_conv[di, dj])
    h = h_in + acc
    return x_frozen + torch.einsum("bhwc,cd->bhwd", h, w_z2)
```

```cpp
#include <hip/hip_runtime.h>
#include <hip/hip_bf16.h>

typedef __attribute__((ext_vector_type(8))) __bf16 bf16x8;
typedef __attribute__((ext_vector_type(2))) __bf16 bf16x2;
typedef __attribute__((ext_vector_type(4))) short s16x4;
typedef __attribute__((ext_vector_type(8))) short s16x8;
typedef __attribute__((ext_vector_type(4))) float f32x4;
typedef __attribute__((ext_vector_type(2))) float f32x2;
typedef __attribute__((ext_vector_type(4))) unsigned u32x4;
typedef __attribute__((address_space(3))) char lds_char;
typedef __attribute__((address_space(3))) void lds_void;

#define DEVINL __device__ __forceinline__

constexpr int PLANE     = 3072;
constexpr int H_OFF     = 0;
constexpr int RING_OFF  = 49152;
constexpr int SLOT      = 24576;
constexpr int C_OFF     = RING_OFF + SLOT;
constexpr int WZ1_OFF   = RING_OFF + 3 * SLOT;
constexpr int STATS_OFF = WZ1_OFF + 32768;
constexpr int LDS_BYTES = STATS_OFF + 6144;
constexpr int WZ2_OFF   = RING_OFF;
constexpr int HOUT_OFF  = RING_OFF + 2 * SLOT;
constexpr int HOUT_PLANE = 2048;
constexpr unsigned OOB  = 0x40000000u;
constexpr int TENSOR_BYTES = 2 * 128 * 128 * 128 * 2;

#define WAIT_VM(n) asm volatile("s_waitcnt vmcnt(" #n ")" ::: "memory")
#define BAR() asm volatile("s_waitcnt lgkmcnt(0)\n\ts_barrier" ::: "memory")

DEVINL bf16x8 lds_b128(const lds_char* p) { return *(const __attribute__((address_space(3))) bf16x8*)p; }
DEVINL bf16x8 lds_tr_frag(const lds_char* p) {
  s16x4 lo = __builtin_amdgcn_ds_read_tr16_b64_v4i16((__attribute__((address_space(3))) s16x4*)p);
  s16x4 hi = __builtin_amdgcn_ds_read_tr16_b64_v4i16((__attribute__((address_space(3))) s16x4*)(p + 1024));
  s16x8 r = __builtin_shufflevector(lo, hi, 0, 1, 2, 3, 4, 5, 6, 7);
  return __builtin_bit_cast(bf16x8, r);
}
DEVINL f32x4 mfma(bf16x8 a, bf16x8 b, f32x4 c) { return __builtin_amdgcn_mfma_f32_16x16x32_bf16(a, b, c, 0, 0, 0); }
DEVINL unsigned pk2(float lo, float hi) {
  f32x2 f = {lo, hi};
  return __builtin_bit_cast(unsigned, __builtin_convertvector(f, bf16x2));
}
DEVINL float bf_lo(unsigned u) { return __uint_as_float(u << 16); }
DEVINL float bf_hi(unsigned u) { return __uint_as_float(u & 0xffff0000u); }
DEVINL void unpack_chunk(u32x4 L, f32x4& t0, f32x4& t1) {
  auto a = __builtin_amdgcn_permlane16_swap(L[0], L[2], false, false);
  auto b = __builtin_amdgcn_permlane16_swap(L[1], L[3], false, false);
  t0[0] = bf_lo(a[0]); t0[1] = bf_hi(a[0]); t0[2] = bf_lo(b[0]); t0[3] = bf_hi(b[0]);
  t1[0] = bf_lo(a[1]); t1[1] = bf_hi(a[1]); t1[2] = bf_lo(b[1]); t1[3] = bf_hi(b[1]);
}
DEVINL u32x4 pack_chunk(f32x4 t0, f32x4 t1) {
  unsigned p00 = pk2(t0[0], t0[1]), p01 = pk2(t0[2], t0[3]);
  unsigned p10 = pk2(t1[0], t1[1]), p11 = pk2(t1[2], t1[3]);
  auto a = __builtin_amdgcn_permlane16_swap(p00, p10, false, false);
  auto b = __builtin_amdgcn_permlane16_swap(p01, p11, false, false);
  u32x4 r = {a[0], b[0], a[1], b[1]};
  return r;
}
DEVINL float reduce4(float a, float b, float c, float d) {
  auto r0 = __builtin_amdgcn_permlane16_swap(__float_as_uint(a), __float_as_uint(b), false, false);
  auto r1 = __builtin_amdgcn_permlane16_swap(__float_as_uint(c), __float_as_uint(d), false, false);
  float ab = __uint_as_float(r0[0]) + __uint_as_float(r0[1]);
  float cd = __uint_as_float(r1[0]) + __uint_as_float(r1[1]);
  auto r2 = __builtin_amdgcn_permlane32_swap(__float_as_uint(ab), __float_as_uint(cd), false, false);
  return __uint_as_float(r2[0]) + __uint_as_float(r2[1]);
}

__global__ __launch_bounds__(512, 2)
void controlnet_fused(const __hip_bfloat16* __restrict__ xf, const __hip_bfloat16* __restrict__ x,
                      const __hip_bfloat16* __restrict__ c, const __hip_bfloat16* __restrict__ wz1,
                      const __hip_bfloat16* __restrict__ wz2, const __hip_bfloat16* __restrict__ wconv,
                      __hip_bfloat16* __restrict__ out) {
  extern __shared__ __attribute__((aligned(16))) char smem_generic[];
  lds_char* smem = (lds_char*)smem_generic;

  const int tid = threadIdx.x;
  const int lane = tid & 63;
  const int wv = __builtin_amdgcn_readfirstlane(tid >> 6);
  const int T = wv >> 2, w = wv & 3;
  const int q = lane >> 4, px = lane & 15;

  const int bid = blockIdx.x;
  const int tile = (bid & 7) * 32 + (bid >> 3);
  const int bimg = tile >> 7, ty0 = ((tile >> 3) & 15) * 8, tx0 = (tile & 7) * 16;

  __amdgpu_buffer_rsrc_t r_c  = __builtin_amdgcn_make_buffer_rsrc((void*)c, 0, TENSOR_BYTES, 0x00020000);
  __amdgpu_buffer_rsrc_t r_x  = __builtin_amdgcn_make_buffer_rsrc((void*)x, 0, TENSOR_BYTES, 0x00020000);
  __amdgpu_buffer_rsrc_t r_xf = __builtin_amdgcn_make_buffer_rsrc((void*)xf, 0, TENSOR_BYTES, 0x00020000);
  __amdgpu_buffer_rsrc_t r_o  = __builtin_amdgcn_make_buffer_rsrc((void*)out, 0, TENSOR_BYTES, 0x00020000);
  __amdgpu_buffer_rsrc_t r_w1 = __builtin_amdgcn_make_buffer_rsrc((void*)wz1, 0, 32768, 0x00020000);
  __amdgpu_buffer_rsrc_t r_w2 = __builtin_amdgcn_make_buffer_rsrc((void*)wz2, 0, 32768, 0x00020000);
  __amdgpu_buffer_rsrc_t r_wc = __builtin_amdgcn_make_buffer_rsrc((void*)wconv, 0, 9 * 32768, 0x00020000);

  auto pix_off = [&](int r, int cc) -> unsigned {
    int gy = ty0 - 1 + r, gx = tx0 - 1 + cc;
    bool ok = ((unsigned)gy < 128u) && ((unsigned)gx < 128u);
    return ok ? (unsigned)(((bimg * 128 + gy) * 128 + gx) * 256) : OOB;
  };
  auto grp_rc = [&](int j, int p, int& r, int& cc, bool& ok) {
    if (j < 5) { r = (T == 0) ? j : (j == 0 ? 9 : j + 4); cc = p + 1; ok = true; }
    else { r = 5 * T + (p >> 1); cc = (p & 1) ? 17 : 0; ok = p < 10; }
  };

  const int wk = 4 * wv + q;
  const int wf = (wk & 3) | (((wk >> 3) & 1) << 2);
  const unsigned wvoff = (unsigned)(wk * 256 + ((px ^ (wf << 1)) * 16));
  auto dma_block = [&](__amdgpu_buffer_rsrc_t rs, int src_off, int lds_off) {
    __builtin_amdgcn_raw_ptr_buffer_load_lds(rs, (lds_void*)(smem + lds_off + wv * 1024), 16, wvoff, src_off, 0, 0);
  };
  auto dma_stage = [&](int s) {
    const int dj = s >> 2, ks = s & 3;
#pragma unroll
    for (int di = 0; di < 3; ++di)
      dma_block(r_wc, (di * 3 + dj) * 32768 + ks * 8192, RING_OFF + (s & 3) * SLOT + di * 8192);
  };

#pragma unroll
  for (int ks = 0; ks < 4; ++ks) dma_block(r_w1, ks * 8192, WZ1_OFF + ks * 8192);
#pragma unroll
  for (int j = 0; j < 6; ++j) {
    int r, cc; bool ok; const int p = 4 * w + q;
    grp_rc(j, p, r, cc, ok);
    unsigned o = ok ? pix_off(r, cc) : OOB;
    o += (unsigned)((px ^ p) * 16);
    __builtin_amdgcn_raw_ptr_buffer_load_lds(r_c, (lds_void*)(smem + C_OFF + ((T * 6 + j) * 16 + 4 * w) * 256), 16, o, 0, 0, 0);
  }
  const int sq = ((q & 1) << 1) | (q >> 1);
  u32x4 xr[6];
  int hpix[6];
#pragma unroll
  for (int j = 0; j < 6; ++j) {
    int r, cc; bool ok;
    grp_rc(j, px, r, cc, ok);
    unsigned o = ok ? pix_off(r, cc) : OOB;
    hpix[j] = ok ? r * 18 + cc : 180 + 6 * T + (px - 10);
    xr[j] = __builtin_amdgcn_raw_buffer_load_b128(r_x, o + (unsigned)((4 * w + sq) * 16), 0, 0);
  }
  WAIT_VM(0);
  BAR();
  dma_stage(0);

  const int aq = (lane & 15) >> 2, ap = lane & 3;
  const int af = aq | ((q & 1) << 2);
  const int abase0 = (8 * q + aq) * 256 + (((2 * w) ^ af) * 32) + 8 * ap;
  const int abase1 = abase0 ^ 32;

  f32x4 hin[6][2];
#pragma unroll
  for (int j = 0; j < 6; ++j) unpack_chunk(xr[j], hin[j][0], hin[j][1]);
  {
    bf16x8 A[4][2];
#pragma unroll
    for (int ks = 0; ks < 4; ++ks) {
      A[ks][0] = lds_tr_frag(smem + WZ1_OFF + ks * 8192 + abase0);
      A[ks][1] = lds_tr_frag(smem + WZ1_OFF + ks * 8192 + abase1);
    }
#pragma unroll
    for (int j = 0; j < 6; ++j) {
#pragma unroll
      for (int ks = 0; ks < 4; ++ks) {
        bf16x8 B = lds_b128(smem + C_OFF + ((T * 6 + j) * 16 + px) * 256 + (((4 * ks + q) ^ px) * 16));
        hin[j][0] = mfma(A[ks][0], B, hin[j][0]);
        hin[j][1] = mfma(A[ks][1], B, hin[j][1]);
      }
    }
  }
  {
    float s[6], ss[6];
#pragma unroll
    for (int j = 0; j < 6; ++j) {
      float a = 0.f, b = 0.f;
#pragma unroll
      for (int t = 0; t < 2; ++t)
#pragma unroll
        for (int e = 0; e < 4; ++e) { float v = hin[j][t][e]; a += v; b = fmaf(v, v, b); }
      s[j] = a; ss[j] = b;
    }
#pragma unroll
    for (int m = 0; m < 3; ++m) {
      float r = reduce4(s[2 * m], ss[2 * m], s[2 * m + 1], ss[2 * m + 1]);
      const int g = 2 * m + (q >> 1);
      *(__attribute__((address_space(3))) float*)(smem + STATS_OFF + (w >> 1) * 3072 + ((T * 6 + g) * 16 + px) * 16 + (w & 1) * 8 + (q & 1) * 4) = r;
    }
  }
  WAIT_VM(0);
  BAR();
  u32x4 xfr[4];
#pragma unroll
  for (int i = 0; i < 4; ++i) {
    unsigned o = (unsigned)(((bimg * 128 + ty0 + 4 * T + i) * 128 + tx0 + px) * 256 + (4 * w + sq) * 16);
    xfr[i] = __builtin_amdgcn_raw_buffer_load_b128(r_xf, o, 0, 0);
  }
  dma_stage(1); dma_stage(2); dma_stage(3);

  bf16x8 Af[2][3][2], Bf[2][6];
#pragma unroll
  for (int di = 0; di < 3; ++di) {
    Af[0][di][0] = lds_tr_frag(smem + RING_OFF + di * 8192 + abase0);
    Af[0][di][1] = lds_tr_frag(smem + RING_OFF + di * 8192 + abase1);
  }
#pragma unroll
  for (int j = 0; j < 6; ++j) {
    const lds_char* sp = smem + STATS_OFF + ((T * 6 + j) * 16 + px) * 16;
    f32x4 p0 = *(const __attribute__((address_space(3))) f32x4*)(sp);
    f32x4 p1 = *(const __attribute__((address_space(3))) f32x4*)(sp + 3072);
    float sum = (p0[0] + p0[2]) + (p1[0] + p1[2]);
    float sq2 = (p0[1] + p0[3]) + (p1[1] + p1[3]);
    float mean = sum * (1.f / 128.f);
    float var = fmaxf(sq2 * (1.f / 128.f) - mean * mean, 0.f);
    float rstd = __builtin_amdgcn_rsqf(var + 1e-5f);
    float nb = -mean * rstd;
    f32x4 hs[2];
#pragma unroll
    for (int t = 0; t < 2; ++t)
#pragma unroll
      for (int e = 0; e < 4; ++e) {
        float y = fmaf(hin[j][t][e], rstd, nb);
        float ex = __builtin_amdgcn_exp2f(y * -1.4426950408889634f);
        hs[t][e] = y * __builtin_amdgcn_rcpf(1.f + ex);
      }
    u32x4 pk = pack_chunk(hs[0], hs[1]);
    *(__attribute__((address_space(3))) u32x4*)(smem + H_OFF + (4 * w + sq) * PLANE + hpix[j] * 16) = pk;
  }
  f32x4 acc[4][2];
#pragma unroll
  for (int i = 0; i < 4; ++i) { acc[i][0] = hin[i + 1][0]; acc[i][1] = hin[i + 1][1]; }

  WAIT_VM(6);
  BAR();

  const lds_char* hb = smem + H_OFF + q * PLANE + (T * 4 * 18 + px) * 16;
#pragma unroll
  for (int rr = 0; rr < 6; ++rr) Bf[0][rr] = lds_b128(hb + rr * 18 * 16);
#pragma unroll
  for (int s = 0; s < 12; ++s) {
    const int cur = s & 1, nxt = cur ^ 1;
    if (s > 0) {
      if (s <= 9) WAIT_VM(6); else if (s == 10) WAIT_VM(4);
      BAR();
    }
    if (s + 4 < 12) dma_stage(s + 4);
    else if (s == 8) { dma_block(r_w2, 0, WZ2_OFF); dma_block(r_w2, 8192, WZ2_OFF + 8192); dma_block(r_w2, 16384, WZ2_OFF + 16384); }
    else if (s == 9) { dma_block(r_w2, 24576, WZ2_OFF + 24576); }
    if (s + 1 < 12) {
      const int s1 = s + 1, dj = s1 >> 2, ks = s1 & 3;
#pragma unroll
      for (int di = 0; di < 3; ++di) {
        Af[nxt][di][0] = lds_tr_frag(smem + RING_OFF + (s1 & 3) * SLOT + di * 8192 + abase0);
        Af[nxt][di][1] = lds_tr_frag(smem + RING_OFF + (s1 & 3) * SLOT + di * 8192 + abase1);
      }
#pragma unroll
      for (int rr = 0; rr < 6; ++rr) Bf[nxt][rr] = lds_b128(hb + ks * 4 * PLANE + (rr * 18 + dj) * 16);
    }
#pragma unroll
    for (int di = 0; di < 3; ++di)
#pragma unroll
      for (int i = 0; i < 4; ++i) {
        acc[i][0] = mfma(Af[cur][di][0], Bf[cur][i + di], acc[i][0]);
        acc[i][1] = mfma(Af[cur][di][1], Bf[cur][i + di], acc[i][1]);
      }
  }
#pragma unroll
  for (int i = 0; i < 4; ++i) {
    u32x4 pk = pack_chunk(acc[i][0], acc[i][1]);
    *(__attribute__((address_space(3))) u32x4*)(smem + HOUT_OFF + (4 * w + sq) * HOUT_PLANE + ((4 * T + i) * 16 + px) * 16) = pk;
  }
  WAIT_VM(0);
  BAR();
  f32x4 o2[4][2];
#pragma unroll
  for (int i = 0; i < 4; ++i) unpack_chunk(xfr[i], o2[i][0], o2[i][1]);
#pragma unroll
  for (int ks = 0; ks < 4; ++ks) {
    bf16x8 A0 = lds_tr_frag(smem + WZ2_OFF + ks * 8192 + abase0);
    bf16x8 A1 = lds_tr_frag(smem + WZ2_OFF + ks * 8192 + abase1);
#pragma unroll
    for (int i = 0; i < 4; ++i) {
      bf16x8 B = lds_b128(smem + HOUT_OFF + (4 * ks + q) * HOUT_PLANE + ((4 * T + i) * 16 + px) * 16);
      o2[i][0] = mfma(A0, B, o2[i][0]);
      o2[i][1] = mfma(A1, B, o2[i][1]);
    }
  }
#pragma unroll
  for (int i = 0; i < 4; ++i) {
    u32x4 pk = pack_chunk(o2[i][0], o2[i][1]);
    unsigned o = (unsigned)(((bimg * 128 + ty0 + 4 * T + i) * 128 + tx0 + px) * 256 + (4 * w + sq) * 16);
    __builtin_amdgcn_raw_buffer_store_b128(pk, r_o, o, 0, 0);
  }
}

extern "C" void kernel_launch(void* const* d_in, const int* in_sizes, int n_in,
                              void* d_out, int out_size, void* d_ws, size_t ws_size,
                              hipStream_t stream) {
  const __hip_bfloat16* xf  = (const __hip_bfloat16*)d_in[0];
  const __hip_bfloat16* x   = (const __hip_bfloat16*)d_in[1];
  const __hip_bfloat16* c   = (const __hip_bfloat16*)d_in[2];
  const __hip_bfloat16* wz1 = (const __hip_bfloat16*)d_in[3];
  const __hip_bfloat16* wz2 = (const __hip_bfloat16*)d_in[4];
  const __hip_bfloat16* wc  = (const __hip_bfloat16*)d_in[5];
  (void)hipFuncSetAttribute((const void*)controlnet_fused, hipFuncAttributeMaxDynamicSharedMemorySize, LDS_BYTES);
  controlnet_fused<<<256, 512, LDS_BYTES, stream>>>(xf, x, c, wz1, wz2, wc, (__hip_bfloat16*)d_out);
}
```

```cpp
#include <hip/hip_runtime.h>
#include <hip/hip_bf16.h>

typedef __attribute__((ext_vector_type(8))) __bf16 bf16x8;
typedef __attribute__((ext_vector_type(2))) __bf16 bf16x2;
typedef __attribute__((ext_vector_type(4))) short s16x4;
typedef __attribute__((ext_vector_type(8))) short s16x8;
typedef __attribute__((ext_vector_type(4))) float f32x4;
typedef __attribute__((ext_vector_type(2))) float f32x2;
typedef __attribute__((ext_vector_type(4))) unsigned u32x4;
typedef __attribute__((address_space(3))) char lds_char;

#define DEVINL __device__ __forceinline__

constexpr int PLANE     = 3072;
constexpr int H_OFF     = 0;
constexpr int RING_OFF  = 49152;
constexpr int SLOT      = 24576;
constexpr int C_OFF     = RING_OFF + SLOT;
constexpr int WZ1_OFF   = RING_OFF + 3 * SLOT;
constexpr int STATS_OFF = WZ1_OFF + 32768;
constexpr int LDS_BYTES = STATS_OFF + 6144;
constexpr int WZ2_OFF   = RING_OFF;
constexpr int HOUT_OFF  = RING_OFF + 2 * SLOT;
constexpr int HOUT_PLANE = 2048;
constexpr unsigned OOB  = 0x40000000u;
constexpr int TENSOR_BYTES = 2 * 128 * 128 * 128 * 2;

#define STAMP(i) do {} while (0)
#define WAIT_VM(n) asm volatile("s_waitcnt vmcnt(" #n ")" ::: "memory")
#define BAR() asm volatile("s_waitcnt lgkmcnt(0)\n\ts_barrier" ::: "memory")
#define SCHED() __builtin_amdgcn_sched_barrier(0)

DEVINL u32x4 make_rsrc(const void* p, unsigned bytes) {
  unsigned long long a = (unsigned long long)p;
  u32x4 r = {(unsigned)a, (unsigned)(a >> 32) & 0xffffu, bytes, 0x00020000u};
  return r;
}
DEVINL void dma16(u32x4 rsrc, unsigned voff, unsigned soff, unsigned lds_addr) {
  asm volatile("s_mov_b32 m0, %0\n\ts_nop 0\n\tbuffer_load_dwordx4 %1, %2, %3 offen lds" :: "s"(lds_addr), "v"(voff), "s"(rsrc), "s"(soff) : "memory");
}
DEVINL void dma16_nt(u32x4 rsrc, unsigned voff, unsigned soff, unsigned lds_addr) {
  asm volatile("s_mov_b32 m0, %0\n\ts_nop 0\n\tbuffer_load_dwordx4 %1, %2, %3 offen nt lds" :: "s"(lds_addr), "v"(voff), "s"(rsrc), "s"(soff) : "memory");
}
DEVINL u32x4 ld16_nt(u32x4 rsrc, unsigned voff) {
  u32x4 r;
  asm volatile("buffer_load_dwordx4 %0, %1, %2, 0 offen nt" : "=v"(r) : "v"(voff), "s"(rsrc) : "memory");
  return r;
}
DEVINL bf16x8 lds_b128(const lds_char* p) { return *(const __attribute__((address_space(3))) bf16x8*)p; }
DEVINL bf16x8 lds_tr_frag(const lds_char* p) {
  s16x4 lo = __builtin_amdgcn_ds_read_tr16_b64_v4i16((__attribute__((address_space(3))) s16x4*)p);
  s16x4 hi = __builtin_amdgcn_ds_read_tr16_b64_v4i16((__attribute__((address_space(3))) s16x4*)(p + 1024));
  s16x8 r = __builtin_shufflevector(lo, hi, 0, 1, 2, 3, 4, 5, 6, 7);
  return __builtin_bit_cast(bf16x8, r);
}
DEVINL f32x4 mfma(bf16x8 a, bf16x8 b, f32x4 c) { return __builtin_amdgcn_mfma_f32_16x16x32_bf16(a, b, c, 0, 0, 0); }
DEVINL unsigned pk2(float lo, float hi) {
  f32x2 f = {lo, hi};
  return __builtin_bit_cast(unsigned, __builtin_convertvector(f, bf16x2));
}
DEVINL float bf_lo(unsigned u) { return __uint_as_float(u << 16); }
DEVINL float bf_hi(unsigned u) { return __uint_as_float(u & 0xffff0000u); }
DEVINL void unpack_chunk(u32x4 L, f32x4& t0, f32x4& t1) {
  auto a = __builtin_amdgcn_permlane16_swap(L[0], L[2], false, false);
  auto b = __builtin_amdgcn_permlane16_swap(L[1], L[3], false, false);
  t0[0] = bf_lo(a[0]); t0[1] = bf_hi(a[0]); t0[2] = bf_lo(b[0]); t0[3] = bf_hi(b[0]);
  t1[0] = bf_lo(a[1]); t1[1] = bf_hi(a[1]); t1[2] = bf_lo(b[1]); t1[3] = bf_hi(b[1]);
}
DEVINL u32x4 pack_chunk(f32x4 t0, f32x4 t1) {
  unsigned p00 = pk2(t0[0], t0[1]), p01 = pk2(t0[2], t0[3]);
  unsigned p10 = pk2(t1[0], t1[1]), p11 = pk2(t1[2], t1[3]);
  auto a = __builtin_amdgcn_permlane16_swap(p00, p10, false, false);
  auto b = __builtin_amdgcn_permlane16_swap(p01, p11, false, false);
  u32x4 r = {a[0], b[0], a[1], b[1]};
  return r;
}
DEVINL float reduce4(float a, float b, float c, float d) {
  auto r0 = __builtin_amdgcn_permlane16_swap(__float_as_uint(a), __float_as_uint(b), false, false);
  auto r1 = __builtin_amdgcn_permlane16_swap(__float_as_uint(c), __float_as_uint(d), false, false);
  float ab = __uint_as_float(r0[0]) + __uint_as_float(r0[1]);
  float cd = __uint_as_float(r1[0]) + __uint_as_float(r1[1]);
  auto r2 = __builtin_amdgcn_permlane32_swap(__float_as_uint(ab), __float_as_uint(cd), false, false);
  return __uint_as_float(r2[0]) + __uint_as_float(r2[1]);
}

__global__ __launch_bounds__(512, 2)
void controlnet_fused(const __hip_bfloat16* __restrict__ xf, const __hip_bfloat16* __restrict__ x,
                      const __hip_bfloat16* __restrict__ c, const __hip_bfloat16* __restrict__ wz1,
                      const __hip_bfloat16* __restrict__ wz2, const __hip_bfloat16* __restrict__ wconv,
                      __hip_bfloat16* __restrict__ out
                      ) {
  extern __shared__ __attribute__((aligned(16))) char smem_generic[];
  lds_char* smem = (lds_char*)smem_generic;
  const unsigned lds0 = (unsigned)(size_t)smem;

  const int tid = threadIdx.x;
  const int lane = tid & 63;
  const int wv = __builtin_amdgcn_readfirstlane(tid >> 6);
  const int T = wv >> 2, w = wv & 3;
  const int q = lane >> 4, px = lane & 15;
  STAMP(0);

  const int bid = blockIdx.x;
  const int tile = (bid & 7) * 32 + (bid >> 3);
  const int bimg = tile >> 7, ty0 = ((tile >> 3) & 15) * 8, tx0 = (tile & 7) * 16;

  const u32x4 r_c  = make_rsrc(c, TENSOR_BYTES), r_x = make_rsrc(x, TENSOR_BYTES), r_xf = make_rsrc(xf, TENSOR_BYTES);
  const u32x4 r_w1 = make_rsrc(wz1, 32768), r_w2 = make_rsrc(wz2, 32768), r_wc = make_rsrc(wconv, 9 * 32768);
  __amdgpu_buffer_rsrc_t r_o  = __builtin_amdgcn_make_buffer_rsrc((void*)out, 0, TENSOR_BYTES, 0x00020000);

  auto pix_off = [&](int r, int cc) -> unsigned {
    int gy = ty0 - 1 + r, gx = tx0 - 1 + cc;
    bool ok = ((unsigned)gy < 128u) && ((unsigned)gx < 128u);
    return ok ? (unsigned)(((bimg * 128 + gy) * 128 + gx) * 256) : OOB;
  };
  auto grp_rc = [&](int j, int p, int& r, int& cc, bool& ok) {
    if (j < 5) { r = (T == 0) ? j : (j == 0 ? 9 : j + 4); cc = p + 1; ok = true; }
    else { r = 5 * T + (p >> 1); cc = (p & 1) ? 17 : 0; ok = p < 10; }
  };

  const int wk = 4 * wv + q;
  const int wf = (wk & 3) | (((wk >> 3) & 1) << 2);
  const unsigned wvoff = (unsigned)(wk * 256 + ((px ^ (wf << 1)) * 16));
  const unsigned wlds = lds0 + wv * 1024;
  auto dma_stage = [&](int s) {
    const int dj = s >> 2, ks = s & 3;
#pragma unroll
    for (int di = 0; di < 3; ++di)
      dma16(r_wc, wvoff, (di * 3 + dj) * 32768 + ks * 8192, wlds + RING_OFF + (s & 3) * SLOT + di * 8192);
  };

#pragma unroll
  for (int ks = 0; ks < 4; ++ks) dma16(r_w1, wvoff, ks * 8192, wlds + WZ1_OFF + ks * 8192);
#pragma unroll
  for (int j = 0; j < 6; ++j) {
    int r, cc; bool ok; const int p = 4 * w + q;
    grp_rc(j, p, r, cc, ok);
    unsigned o = ok ? pix_off(r, cc) : OOB;
    o += (unsigned)((px ^ p) * 16);
    dma16_nt(r_c, o, 0, lds0 + C_OFF + ((T * 6 + j) * 16 + 4 * w) * 256);
  }
  const int sq = ((q & 1) << 1) | (q >> 1);
  u32x4 xr[6];
  int hpix[6];
#pragma unroll
  for (int j = 0; j < 6; ++j) {
    int r, cc; bool ok;
    grp_rc(j, px, r, cc, ok);
    unsigned o = ok ? pix_off(r, cc) : OOB;
    hpix[j] = ok ? r * 18 + cc : 180 + 6 * T + (px - 10);
    xr[j] = ld16_nt(r_x, o + (unsigned)((4 * w + sq) * 16));
  }
  dma_stage(0);
  unsigned warm0, warm1;
  {
    const int sl = bid >> 3;
    unsigned o = tid < 72 ? (unsigned)(sl * 9216 + tid * 128) : OOB;
    unsigned o2 = tid < 8 ? (unsigned)(sl * 1024 + tid * 128) : OOB;
    asm volatile("buffer_load_dword %0, %1, %2, 0 offen" : "=v"(warm0) : "v"(o), "s"(r_wc) : "memory");
    asm volatile("buffer_load_dword %0, %1, %2, 0 offen" : "=v"(warm1) : "v"(o2), "s"(r_w2) : "memory");
  }
  asm volatile("s_waitcnt vmcnt(0)" : "+v"(xr[0]), "+v"(xr[1]), "+v"(xr[2]), "+v"(xr[3]), "+v"(xr[4]), "+v"(xr[5]), "+v"(warm0), "+v"(warm1) :: "memory");
  BAR();
  STAMP(1);

  const int aq = (lane & 15) >> 2, ap = lane & 3;
  const int af = aq | ((q & 1) << 2);
  const int abase0 = (8 * q + aq) * 256 + (((2 * w) ^ af) * 32) + 8 * ap;
  const int abase1 = abase0 ^ 32;

  f32x4 hin[6][2];
#pragma unroll
  for (int j = 0; j < 6; ++j) unpack_chunk(xr[j], hin[j][0], hin[j][1]);
  {
    bf16x8 A[4][2];
#pragma unroll
    for (int ks = 0; ks < 4; ++ks) {
      A[ks][0] = lds_tr_frag(smem + WZ1_OFF + ks * 8192 + abase0);
      A[ks][1] = lds_tr_frag(smem + WZ1_OFF + ks * 8192 + abase1);
    }
#pragma unroll
    for (int j = 0; j < 6; ++j) {
#pragma unroll
      for (int ks = 0; ks < 4; ++ks) {
        bf16x8 B = lds_b128(smem + C_OFF + ((T * 6 + j) * 16 + px) * 256 + (((4 * ks + q) ^ px) * 16));
        hin[j][0] = mfma(A[ks][0], B, hin[j][0]);
        hin[j][1] = mfma(A[ks][1], B, hin[j][1]);
      }
    }
  }
  {
    float s[6], ss[6];
#pragma unroll
    for (int j = 0; j < 6; ++j) {
      float a = 0.f, b = 0.f;
#pragma unroll
      for (int t = 0; t < 2; ++t)
#pragma unroll
        for (int e = 0; e < 4; ++e) { float v = hin[j][t][e]; a += v; b = fmaf(v, v, b); }
      s[j] = a; ss[j] = b;
    }
#pragma unroll
    for (int m = 0; m < 3; ++m) {
      float r = reduce4(s[2 * m], ss[2 * m], s[2 * m + 1], ss[2 * m + 1]);
      const int g = 2 * m + (q >> 1);
      *(__attribute__((address_space(3))) float*)(smem + STATS_OFF + (w >> 1) * 3072 + ((T * 6 + g) * 16 + px) * 16 + (w & 1) * 8 + (q & 1) * 4) = r;
    }
  }
  STAMP(2);
  BAR();
  STAMP(3);
  u32x4 xfr[4];
#pragma unroll
  for (int i = 0; i < 4; ++i) {
    unsigned o = (unsigned)(((bimg * 128 + ty0 + 4 * T + i) * 128 + tx0 + px) * 256 + (4 * w + sq) * 16);
    xfr[i] = ld16_nt(r_xf, o);
  }
  dma_stage(1); dma_stage(2); dma_stage(3);

  bf16x8 Af[2][3][2], Bf[2][6];
#pragma unroll
  for (int di = 0; di < 3; ++di) {
    Af[0][di][0] = lds_tr_frag(smem + RING_OFF + di * 8192 + abase0);
    Af[0][di][1] = lds_tr_frag(smem + RING_OFF + di * 8192 + abase1);
  }
#pragma unroll
  for (int j = 0; j < 6; ++j) {
    const lds_char* sp = smem + STATS_OFF + ((T * 6 + j) * 16 + px) * 16;
    f32x4 p0 = *(const __attribute__((address_space(3))) f32x4*)(sp);
    f32x4 p1 = *(const __attribute__((address_space(3))) f32x4*)(sp + 3072);
    float sum = (p0[0] + p0[2]) + (p1[0] + p1[2]);
    float sq2 = (p0[1] + p0[3]) + (p1[1] + p1[3]);
    float mean = sum * (1.f / 128.f);
    float var = fmaxf(sq2 * (1.f / 128.f) - mean * mean, 0.f);
    float rstd = __builtin_amdgcn_rsqf(var + 1e-5f);
    float nb = -mean * rstd;
    f32x4 hs[2];
#pragma unroll
    for (int t = 0; t < 2; ++t)
#pragma unroll
      for (int e = 0; e < 4; ++e) {
        float y = fmaf(hin[j][t][e], rstd, nb);
        float ex = __builtin_amdgcn_exp2f(y * -1.4426950408889634f);
        hs[t][e] = y * __builtin_amdgcn_rcpf(1.f + ex);
      }
    u32x4 pk = pack_chunk(hs[0], hs[1]);
    *(__attribute__((address_space(3))) u32x4*)(smem + H_OFF + (4 * w + sq) * PLANE + hpix[j] * 16) = pk;
  }
  f32x4 acc[4][2];
#pragma unroll
  for (int i = 0; i < 4; ++i) { acc[i][0] = hin[i + 1][0]; acc[i][1] = hin[i + 1][1]; }

  STAMP(4);
  WAIT_VM(6);
  BAR();
  STAMP(5);

  const lds_char* hb = smem + H_OFF + q * PLANE + (T * 4 * 18 + px) * 16;
#pragma unroll
  for (int rr = 0; rr < 6; ++rr) Bf[0][rr] = lds_b128(hb + rr * 18 * 16);
#pragma unroll
  for (int s = 0; s < 12; ++s) {
    const int cur = s & 1, nxt = cur ^ 1;
    SCHED();
    if (s > 0) {
      if (s <= 9) WAIT_VM(6); else if (s == 10) WAIT_VM(4);
      BAR();
    }
    if (s + 4 < 12) dma_stage(s + 4);
    else if (s == 8) { dma16(r_w2, wvoff, 0, wlds + WZ2_OFF); dma16(r_w2, wvoff, 8192, wlds + WZ2_OFF + 8192); dma16(r_w2, wvoff, 16384, wlds + WZ2_OFF + 16384); }
    else if (s == 9) { dma16(r_w2, wvoff, 24576, wlds + WZ2_OFF + 24576); }
    SCHED();
    if (s + 1 < 12) {
      const int s1 = s + 1, dj = s1 >> 2, ks = s1 & 3;
#pragma unroll
      for (int di = 0; di < 3; ++di) {
        Af[nxt][di][0] = lds_tr_frag(smem + RING_OFF + (s1 & 3) * SLOT + di * 8192 + abase0);
        Af[nxt][di][1] = lds_tr_frag(smem + RING_OFF + (s1 & 3) * SLOT + di * 8192 + abase1);
      }
#pragma unroll
      for (int rr = 0; rr < 6; ++rr) Bf[nxt][rr] = lds_b128(hb + ks * 4 * PLANE + (rr * 18 + dj) * 16);
    }
#pragma unroll
    for (int di = 0; di < 3; ++di)
#pragma unroll
      for (int i = 0; i < 4; ++i) {
        acc[i][0] = mfma(Af[cur][di][0], Bf[cur][i + di], acc[i][0]);
        acc[i][1] = mfma(Af[cur][di][1], Bf[cur][i + di], acc[i][1]);
      }
    if (s + 1 < 12) {
#pragma unroll
      for (int g = 0; g < 18; ++g) {
        __builtin_amdgcn_sched_group_barrier(0x008, 1, 0);
        __builtin_amdgcn_sched_group_barrier(0x100, 1, 0);
      }
      __builtin_amdgcn_sched_group_barrier(0x008, 6, 0);
    }
  }
  SCHED();
  STAMP(6);
#pragma unroll
  for (int i = 0; i < 4; ++i) {
    u32x4 pk = pack_chunk(acc[i][0], acc[i][1]);
    *(__attribute__((address_space(3))) u32x4*)(smem + HOUT_OFF + (4 * w + sq) * HOUT_PLANE + ((4 * T + i) * 16 + px) * 16) = pk;
  }
  asm volatile("s_waitcnt vmcnt(0)" : "+v"(xfr[0]), "+v"(xfr[1]), "+v"(xfr[2]), "+v"(xfr[3]) :: "memory");
  BAR();
  STAMP(7);
  f32x4 o2[4][2];
#pragma unroll
  for (int i = 0; i < 4; ++i) unpack_chunk(xfr[i], o2[i][0], o2[i][1]);
#pragma unroll
  for (int ks = 0; ks < 4; ++ks) {
    bf16x8 A0 = lds_tr_frag(smem + WZ2_OFF + ks * 8192 + abase0);
    bf16x8 A1 = lds_tr_frag(smem + WZ2_OFF + ks * 8192 + abase1);
#pragma unroll
    for (int i = 0; i < 4; ++i) {
      bf16x8 B = lds_b128(smem + HOUT_OFF + (4 * ks + q) * HOUT_PLANE + ((4 * T + i) * 16 + px) * 16);
      o2[i][0] = mfma(A0, B, o2[i][0]);
      o2[i][1] = mfma(A1, B, o2[i][1]);
    }
  }
#pragma unroll
  for (int i = 0; i < 4; ++i) {
    u32x4 pk = pack_chunk(o2[i][0], o2[i][1]);
    unsigned o = (unsigned)(((bimg * 128 + ty0 + 4 * T + i) * 128 + tx0 + px) * 256 + (4 * w + sq) * 16);
    __builtin_amdgcn_raw_buffer_store_b128(pk, r_o, o, 0, 0);
  }
  STAMP(8);
}

extern "C" void kernel_launch(void* const* d_in, const int* in_sizes, int n_in,
                              void* d_out, int out_size, void* d_ws, size_t ws_size,
                              hipStream_t stream) {
  const __hip_bfloat16* xf  = (const __hip_bfloat16*)d_in[0];
  const __hip_bfloat16* x   = (const __hip_bfloat16*)d_in[1];
  const __hip_bfloat16* c   = (const __hip_bfloat16*)d_in[2];
  const __hip_bfloat16* wz1 = (const __hip_bfloat16*)d_in[3];
  const __hip_bfloat16* wz2 = (const __hip_bfloat16*)d_in[4];
  const __hip_bfloat16* wc  = (const __hip_bfloat16*)d_in[5];
  (void)hipFuncSetAttribute((const void*)controlnet_fused, hipFuncAttributeMaxDynamicSharedMemorySize, LDS_BYTES);
  controlnet_fused<<<256, 512, LDS_BYTES, stream>>>(xf, x, c, wz1, wz2, wc, (__hip_bfloat16*)d_out
  );
}
```

```cpp
#include <hip/hip_runtime.h>
#include <hip/hip_bf16.h>

typedef __attribute__((ext_vector_type(8))) __bf16 bf16x8;
typedef __attribute__((ext_vector_type(2))) __bf16 bf16x2;
typedef __attribute__((ext_vector_type(4))) short s16x4;
typedef __attribute__((ext_vector_type(8))) short s16x8;
typedef __attribute__((ext_vector_type(4))) float f32x4;
typedef __attribute__((ext_vector_type(2))) float f32x2;
typedef __attribute__((ext_vector_type(4))) unsigned u32x4;
typedef __attribute__((ext_vector_type(2))) unsigned u32x2;
typedef __attribute__((address_space(3))) char lds_char;

#define DEVINL __device__ __forceinline__

constexpr int PLANE     = 3072;
constexpr int H_OFF     = 0;
constexpr int SL0       = 49152;
constexpr int C_OFF     = 73728;
constexpr int SL2       = C_OFF, SL3 = C_OFF + 24576;
constexpr int WZ1_OFF   = 122880;
constexpr int SL1       = WZ1_OFF;
constexpr int STATS_OFF = WZ1_OFF + 32768;
constexpr int LDS_BYTES = STATS_OFF + 6144;
constexpr int HOUT_OFF  = C_OFF;
__host__ __device__ constexpr int slot_off(int s) { return (s & 3) == 0 ? SL0 : (s & 3) == 1 ? SL1 : (s & 3) == 2 ? SL2 : SL3; }
__host__ __device__ constexpr int wz2_off(int ks) { return ks < 3 ? SL0 + ks * 8192 : SL1; }
constexpr int HOUT_PLANE = 2048;
constexpr unsigned OOB  = 0x40000000u;
constexpr int TENSOR_BYTES = 2 * 128 * 128 * 128 * 2;

#define STAMP(i) do {} while (0)
#define STORE_AUX 17
#define WAIT_VM(n) asm volatile("s_waitcnt vmcnt(" #n ")" ::: "memory")
#define BAR() asm volatile("s_waitcnt lgkmcnt(0)\n\ts_barrier" ::: "memory")
#define SCHED() __builtin_amdgcn_sched_barrier(0)

template <int N> struct IC { static constexpr int value = N; };
template <int I, int N, class F> DEVINL void static_for(F&& f) { if constexpr (I < N) { f(IC<I>{}); static_for<I + 1, N>(f); } }

DEVINL u32x4 make_rsrc(const void* p, unsigned bytes) {
  unsigned long long a = (unsigned long long)p;
  u32x4 r = {(unsigned)a, (unsigned)(a >> 32) & 0xffffu, bytes, 0x00020000u};
  return r;
}
DEVINL void dma16(u32x4 rsrc, unsigned voff, unsigned soff, unsigned lds_addr) {
  asm volatile("s_mov_b32 m0, %0\n\ts_nop 0\n\tbuffer_load_dwordx4 %1, %2, %3 offen lds" :: "s"(lds_addr), "v"(voff), "s"(rsrc), "s"(soff) : "memory");
}
DEVINL void dma16_nt(u32x4 rsrc, unsigned voff, unsigned soff, unsigned lds_addr) {
  asm volatile("s_mov_b32 m0, %0\n\ts_nop 0\n\tbuffer_load_dwordx4 %1, %2, %3 offen nt lds" :: "s"(lds_addr), "v"(voff), "s"(rsrc), "s"(soff) : "memory");
}
DEVINL u32x4 ld16_nt(u32x4 rsrc, unsigned voff) {
  u32x4 r;
  asm volatile("buffer_load_dwordx4 %0, %1, %2, 0 offen nt" : "=v"(r) : "v"(voff), "s"(rsrc) : "memory");
  return r;
}
DEVINL bf16x8 lds_b128(const lds_char* p) { return *(const __attribute__((address_space(3))) bf16x8*)p; }
DEVINL bf16x8 lds_tr_frag(const lds_char* p) {
  s16x4 lo = __builtin_amdgcn_ds_read_tr16_b64_v4i16((__attribute__((address_space(3))) s16x4*)p);
  s16x4 hi = __builtin_amdgcn_ds_read_tr16_b64_v4i16((__attribute__((address_space(3))) s16x4*)(p + 1024));
  s16x8 r = __builtin_shufflevector(lo, hi, 0, 1, 2, 3, 4, 5, 6, 7);
  return __builtin_bit_cast(bf16x8, r);
}
DEVINL f32x4 mfma(bf16x8 a, bf16x8 b, f32x4 c) { return __builtin_amdgcn_mfma_f32_16x16x32_bf16(a, b, c, 0, 0, 0); }
DEVINL unsigned pk2(float lo, float hi) {
  f32x2 f = {lo, hi};
  return __builtin_bit_cast(unsigned, __builtin_convertvector(f, bf16x2));
}
DEVINL float bf_lo(unsigned u) { return __uint_as_float(u << 16); }
DEVINL float bf_hi(unsigned u) { return __uint_as_float(u & 0xffff0000u); }
DEVINL void unpack_chunk(u32x4 L, f32x4& t0, f32x4& t1) {
  auto a = __builtin_amdgcn_permlane16_swap(L[0], L[2], false, false);
  auto b = __builtin_amdgcn_permlane16_swap(L[1], L[3], false, false);
  t0[0] = bf_lo(a[0]); t0[1] = bf_hi(a[0]); t0[2] = bf_lo(b[0]); t0[3] = bf_hi(b[0]);
  t1[0] = bf_lo(a[1]); t1[1] = bf_hi(a[1]); t1[2] = bf_lo(b[1]); t1[3] = bf_hi(b[1]);
}
DEVINL u32x4 pack_chunk(f32x4 t0, f32x4 t1) {
  unsigned p00 = pk2(t0[0], t0[1]), p01 = pk2(t0[2], t0[3]);
  unsigned p10 = pk2(t1[0], t1[1]), p11 = pk2(t1[2], t1[3]);
  auto a = __builtin_amdgcn_permlane16_swap(p00, p10, false, false);
  auto b = __builtin_amdgcn_permlane16_swap(p01, p11, false, false);
  u32x4 r = {a[0], b[0], a[1], b[1]};
  return r;
}
DEVINL float reduce4(float a, float b, float c, float d) {
  auto r0 = __builtin_amdgcn_permlane16_swap(__float_as_uint(a), __float_as_uint(b), false, false);
  auto r1 = __builtin_amdgcn_permlane16_swap(__float_as_uint(c), __float_as_uint(d), false, false);
  float ab = __uint_as_float(r0[0]) + __uint_as_float(r0[1]);
  float cd = __uint_as_float(r1[0]) + __uint_as_float(r1[1]);
  auto r2 = __builtin_amdgcn_permlane32_swap(__float_as_uint(ab), __float_as_uint(cd), false, false);
  return __uint_as_float(r2[0]) + __uint_as_float(r2[1]);
}

__global__ __launch_bounds__(512, 2)
void controlnet_fused(const __hip_bfloat16* __restrict__ xf, const __hip_bfloat16* __restrict__ x,
                      const __hip_bfloat16* __restrict__ c, const __hip_bfloat16* __restrict__ wz1,
                      const __hip_bfloat16* __restrict__ wz2, const __hip_bfloat16* __restrict__ wconv,
                      __hip_bfloat16* __restrict__ out
                      ) {
  extern __shared__ __attribute__((aligned(16))) char smem_generic[];
  lds_char* smem = (lds_char*)smem_generic;
  const unsigned lds0 = (unsigned)(size_t)smem;

  const int tid = threadIdx.x;
  const int lane = tid & 63;
  const int wv = __builtin_amdgcn_readfirstlane(tid >> 6);
  const int T = wv >> 2, w = wv & 3;
  const int q = lane >> 4, px = lane & 15;
  STAMP(0);

  const int bid = blockIdx.x;
  const int tile = (bid & 7) * 32 + (bid >> 3);
  const int bimg = tile >> 7, ty0 = ((tile >> 3) & 15) * 8, tx0 = (tile & 7) * 16;

  const u32x4 r_c  = make_rsrc(c, TENSOR_BYTES), r_x = make_rsrc(x, TENSOR_BYTES), r_xf = make_rsrc(xf, TENSOR_BYTES);
  const u32x4 r_w1 = make_rsrc(wz1, 32768), r_w2 = make_rsrc(wz2, 32768), r_wc = make_rsrc(wconv, 9 * 32768);
  __amdgpu_buffer_rsrc_t r_o  = __builtin_amdgcn_make_buffer_rsrc((void*)out, 0, TENSOR_BYTES, 0x00020000);

  auto pix_off = [&](int r, int cc) -> unsigned {
    int gy = ty0 - 1 + r, gx = tx0 - 1 + cc;
    bool ok = ((unsigned)gy < 128u) && ((unsigned)gx < 128u);
    return ok ? (unsigned)(((bimg * 128 + gy) * 128 + gx) * 256) : OOB;
  };
  auto grp_rc = [&](int j, int p, int& r, int& cc, bool& ok) {
    if (j < 5) { r = (T == 0) ? j : (j == 0 ? 9 : j + 4); cc = p + 1; ok = true; }
    else { r = 5 * T + (p >> 1); cc = (p & 1) ? 17 : 0; ok = p < 10; }
  };

  const int wk = 4 * wv + q;
  const int wf = (wk & 3) | (((wk >> 3) & 1) << 2);
  const unsigned wvoff = (unsigned)(wk * 256 + ((px ^ (wf << 1)) * 16));
  const unsigned wlds = lds0 + wv * 1024;
  auto dma_stage = [&](int s) {
    const int dj = s >> 2, ks = s & 3;
#pragma unroll
    for (int di = 0; di < 3; ++di)
      dma16(r_wc, wvoff, (di * 3 + dj) * 32768 + ks * 8192, wlds + slot_off(s) + di * 8192);
  };

  unsigned warm0, warm1;
  {
    const int sl = bid >> 3;
    unsigned o = tid < 72 ? (unsigned)(sl * 9216 + tid * 128) : OOB;
    unsigned o2 = tid < 8 ? (unsigned)(sl * 1024 + tid * 128) : OOB;
    asm volatile("buffer_load_dword %0, %1, %2, 0 offen" : "=v"(warm0) : "v"(o), "s"(r_wc) : "memory");
    asm volatile("buffer_load_dword %0, %1, %2, 0 offen" : "=v"(warm1) : "v"(o2), "s"(r_w2) : "memory");
  }
#pragma unroll
  for (int ks = 0; ks < 4; ++ks) dma16(r_w1, wvoff, ks * 8192, wlds + WZ1_OFF + ks * 8192);
  u32x4 xr[6];
  int hpix[6];
#pragma unroll
  for (int k = 0; k < 3; ++k) {
#pragma unroll
    for (int jj = 0; jj < 2; ++jj) {
      const int j = 2 * k + jj;
      int r, cc; bool ok; const int p = 4 * w + q;
      grp_rc(j, p, r, cc, ok);
      unsigned o = ok ? pix_off(r, cc) : OOB;
      o += (unsigned)((px ^ p) * 16);
      dma16_nt(r_c, o, 0, lds0 + C_OFF + ((k * 4 + T * 2 + jj) * 16 + 4 * w) * 256);
    }
#pragma unroll
    for (int jj = 0; jj < 2; ++jj) {
      const int j = 2 * k + jj;
      int r, cc; bool ok;
      grp_rc(j, px, r, cc, ok);
      unsigned o = ok ? pix_off(r, cc) : OOB;
      hpix[j] = ok ? r * 18 + cc : 180 + 6 * T + (px - 10);
      xr[j] = ld16_nt(r_x, o + (unsigned)((4 * w + q) * 16));
    }
  }
  dma_stage(0);

  const int aq = (lane & 15) >> 2, ap = lane & 3;
  const int af = aq | ((q & 1) << 2);
  const int abase0 = (8 * q + aq) * 256 + (((2 * w) ^ af) * 32) + 8 * ap;
  const int abase1 = abase0 ^ 32;
  bf16x8 Id[2];
  {
    const int jj = px & 7;
    const unsigned one = (jj & 1) ? 0x3F800000u : 0x00003F80u;
    const bool on = ((px >> 3) == (q & 1));
    u32x4 v;
#pragma unroll
    for (int d = 0; d < 4; ++d) v[d] = (on && (jj >> 1) == d) ? one : 0u;
    u32x4 z = {0u, 0u, 0u, 0u};
    Id[0] = __builtin_bit_cast(bf16x8, (q >> 1) == 0 ? v : z);
    Id[1] = __builtin_bit_cast(bf16x8, (q >> 1) == 1 ? v : z);
  }
  const f32x4 zero4 = {0.f, 0.f, 0.f, 0.f};
  const int hwbase = H_OFF + (4 * w + (q >> 1)) * PLANE + (q & 1) * 8;

  f32x4 hin[6][2];
  bf16x8 A1f[4][2];
  bf16x8 Af[2][3][2], Bf[2][6];
  u32x4 xfr[4];
  static_for<0, 3>([&](auto kc) __attribute__((always_inline)) {
    constexpr int k = decltype(kc)::value;
    if (k == 0) asm volatile("s_waitcnt vmcnt(11)" : "+v"(xr[0]), "+v"(xr[1]), "+v"(warm0), "+v"(warm1) :: "memory");
    if (k == 1) asm volatile("s_waitcnt vmcnt(10)" : "+v"(xr[2]), "+v"(xr[3]) :: "memory");
    if (k == 2) asm volatile("s_waitcnt vmcnt(9)" : "+v"(xr[4]), "+v"(xr[5]) :: "memory");
    BAR();
    STAMP(1 + k);
    if (k == 0) {
#pragma unroll
      for (int ks = 0; ks < 4; ++ks) {
        A1f[ks][0] = lds_tr_frag(smem + WZ1_OFF + ks * 8192 + abase0);
        A1f[ks][1] = lds_tr_frag(smem + WZ1_OFF + ks * 8192 + abase1);
      }
    }
    float st[4];
#pragma unroll
    for (int jj = 0; jj < 2; ++jj) {
      const int j = 2 * k + jj;
      bf16x8 B[4];
#pragma unroll
      for (int ks = 0; ks < 4; ++ks)
        B[ks] = lds_b128(smem + C_OFF + ((k * 4 + T * 2 + jj) * 16 + px) * 256 + (((4 * ks + q) ^ px) * 16));
      bf16x8 X = __builtin_bit_cast(bf16x8, xr[j]);
      hin[j][0] = mfma(Id[0], X, zero4);
      hin[j][1] = mfma(Id[1], X, zero4);
#pragma unroll
      for (int ks = 0; ks < 4; ++ks) {
        hin[j][0] = mfma(A1f[ks][0], B[ks], hin[j][0]);
        hin[j][1] = mfma(A1f[ks][1], B[ks], hin[j][1]);
      }
    }
#pragma unroll
    for (int jj = 0; jj < 2; ++jj) {
      const int j = 2 * k + jj;
      float a0 = hin[j][0][0] + hin[j][0][1], a1 = hin[j][0][2] + hin[j][0][3];
      float a2 = hin[j][1][0] + hin[j][1][1], a3 = hin[j][1][2] + hin[j][1][3];
      float b0 = hin[j][0][0] * hin[j][0][0], b1 = hin[j][0][2] * hin[j][0][2];
      float b2 = hin[j][1][0] * hin[j][1][0], b3 = hin[j][1][2] * hin[j][1][2];
      b0 = fmaf(hin[j][0][1], hin[j][0][1], b0); b1 = fmaf(hin[j][0][3], hin[j][0][3], b1);
      b2 = fmaf(hin[j][1][1], hin[j][1][1], b2); b3 = fmaf(hin[j][1][3], hin[j][1][3], b3);
      st[2 * jj] = (a0 + a1) + (a2 + a3);
      st[2 * jj + 1] = (b0 + b1) + (b2 + b3);
    }
    {
      float r = reduce4(st[0], st[1], st[2], st[3]);
      const int g = 2 * k + (q >> 1);
      *(__attribute__((address_space(3))) float*)(smem + STATS_OFF + (w >> 1) * 3072 + ((T * 6 + g) * 16 + px) * 16 + (w & 1) * 8 + (q & 1) * 4) = r;
    }
    if (k == 2) WAIT_VM(6);
    BAR();
    STAMP(4 + k);
    f32x4 sp0[2], sp1[2];
#pragma unroll
    for (int jj = 0; jj < 2; ++jj) {
      const lds_char* sp = smem + STATS_OFF + ((T * 6 + 2 * k + jj) * 16 + px) * 16;
      sp0[jj] = *(const __attribute__((address_space(3))) f32x4*)(sp);
      sp1[jj] = *(const __attribute__((address_space(3))) f32x4*)(sp + 3072);
    }
    if (k == 2) {
#pragma unroll
      for (int i = 0; i < 4; ++i) {
        unsigned o = (unsigned)(((bimg * 128 + ty0 + 4 * T + i) * 128 + tx0 + px) * 256 + (4 * w + q) * 16);
        xfr[i] = ld16_nt(r_xf, o);
      }
    }
    dma_stage(k + 1);
    if (k == 2) {
#pragma unroll
      for (int di = 0; di < 3; ++di) {
        Af[0][di][0] = lds_tr_frag(smem + SL0 + di * 8192 + abase0);
        Af[0][di][1] = lds_tr_frag(smem + SL0 + di * 8192 + abase1);
      }
    }
    float rs[2], nb[2];
#pragma unroll
    for (int jj = 0; jj < 2; ++jj) {
      float sum = (sp0[jj][0] + sp0[jj][2]) + (sp1[jj][0] + sp1[jj][2]);
      float sq2 = (sp0[jj][1] + sp0[jj][3]) + (sp1[jj][1] + sp1[jj][3]);
      float mean = sum * (1.f / 128.f);
      float var = fmaxf(fmaf(sq2, 1.f / 128.f, -mean * mean), 0.f);
      rs[jj] = __builtin_amdgcn_rsqf(var + 1e-5f);
      nb[jj] = -mean * rs[jj];
    }
    float y[16], d[16];
#pragma unroll
    for (int v = 0; v < 16; ++v) y[v] = fmaf(hin[2 * k + (v >> 3)][(v >> 2) & 1][v & 3], rs[v >> 3], nb[v >> 3]);
#pragma unroll
    for (int v = 0; v < 16; ++v) d[v] = y[v] * -1.4426950408889634f;
#pragma unroll
    for (int v = 0; v < 16; ++v) d[v] = __builtin_amdgcn_exp2f(d[v]);
#pragma unroll
    for (int v = 0; v < 16; ++v) d[v] += 1.f;
#pragma unroll
    for (int g4 = 0; g4 < 4; ++g4) {
      const int v = 4 * g4;
      float p01 = d[v] * d[v + 1], p23 = d[v + 2] * d[v + 3];
      float rr = __builtin_amdgcn_rcpf(p01 * p23);
      float r01 = rr * p23, r23 = rr * p01;
      y[v] *= d[v + 1]; y[v + 1] *= d[v]; y[v + 2] *= d[v + 3]; y[v + 3] *= d[v + 2];
      y[v] *= r01; y[v + 1] *= r01; y[v + 2] *= r23; y[v + 3] *= r23;
    }
#pragma unroll
    for (int jj = 0; jj < 2; ++jj)
#pragma unroll
      for (int t = 0; t < 2; ++t) {
        const int v = 8 * jj + 4 * t;
        u32x2 pk = {pk2(y[v], y[v + 1]), pk2(y[v + 2], y[v + 3])};
        *(__attribute__((address_space(3))) u32x2*)(smem + hwbase + 2 * t * PLANE + hpix[2 * k + jj] * 16) = pk;
      }
  });
  f32x4 acc[4][2];
#pragma unroll
  for (int i = 0; i < 4; ++i) { acc[i][0] = hin[i + 1][0]; acc[i][1] = hin[i + 1][1]; }

  STAMP(7);
  WAIT_VM(10);
  BAR();
  STAMP(8);

  const lds_char* hb = smem + H_OFF + q * PLANE + (T * 4 * 18 + px) * 16;
#pragma unroll
  for (int rr = 0; rr < 6; ++rr) Bf[0][rr] = lds_b128(hb + rr * 18 * 16);
  bf16x8 A2f[4][2];
  static_for<0, 12>([&](auto sc) __attribute__((always_inline)) {
    constexpr int s = decltype(sc)::value;
    constexpr int cur = s & 1, nxt = cur ^ 1;
    SCHED();
    if constexpr (s > 0) {
      if constexpr (s == 1) WAIT_VM(10); else if constexpr (s <= 9) WAIT_VM(6); else if constexpr (s == 10) WAIT_VM(4); else WAIT_VM(1);
      BAR();
    }
    if constexpr (s + 4 < 12) dma_stage(s + 4);
    else if constexpr (s == 8) { dma16(r_w2, wvoff, 0, wlds + wz2_off(0)); dma16(r_w2, wvoff, 8192, wlds + wz2_off(1)); dma16(r_w2, wvoff, 16384, wlds + wz2_off(2)); }
    else if constexpr (s == 9) { dma16(r_w2, wvoff, 24576, wlds + wz2_off(3)); }
    SCHED();
    if constexpr (s + 1 < 12) {
      constexpr int s1 = s + 1, dj = s1 >> 2, ks = s1 & 3;
#pragma unroll
      for (int di = 0; di < 3; ++di) {
        Af[nxt][di][0] = lds_tr_frag(smem + slot_off(s1) + di * 8192 + abase0);
        Af[nxt][di][1] = lds_tr_frag(smem + slot_off(s1) + di * 8192 + abase1);
      }
#pragma unroll
      for (int rr = 0; rr < 6; ++rr) Bf[nxt][rr] = lds_b128(hb + ks * 4 * PLANE + (rr * 18 + dj) * 16);
    } else {
#pragma unroll
      for (int ks = 0; ks < 3; ++ks) {
        A2f[ks][0] = lds_tr_frag(smem + wz2_off(ks) + abase0);
        A2f[ks][1] = lds_tr_frag(smem + wz2_off(ks) + abase1);
      }
    }
#pragma unroll
    for (int di = 0; di < 3; ++di)
#pragma unroll
      for (int i = 0; i < 4; ++i) {
        acc[i][0] = mfma(Af[cur][di][0], Bf[cur][i + di], acc[i][0]);
        acc[i][1] = mfma(Af[cur][di][1], Bf[cur][i + di], acc[i][1]);
      }
    if constexpr (s + 1 < 12) {
#pragma unroll
      for (int g = 0; g < 18; ++g) {
        __builtin_amdgcn_sched_group_barrier(0x008, 1, 0);
        __builtin_amdgcn_sched_group_barrier(0x100, 1, 0);
      }
      __builtin_amdgcn_sched_group_barrier(0x008, 6, 0);
    } else {
#pragma unroll
      for (int g = 0; g < 12; ++g) {
        __builtin_amdgcn_sched_group_barrier(0x008, 1, 0);
        __builtin_amdgcn_sched_group_barrier(0x100, 1, 0);
      }
      __builtin_amdgcn_sched_group_barrier(0x008, 12, 0);
    }
  });
  SCHED();
  STAMP(9);
  const int owbase = HOUT_OFF + (4 * w + (q >> 1)) * HOUT_PLANE + (q & 1) * 8 + (4 * T * 16 + px) * 16;
#pragma unroll
  for (int i = 0; i < 4; ++i)
#pragma unroll
    for (int t = 0; t < 2; ++t) {
      u32x2 pk = {pk2(acc[i][t][0], acc[i][t][1]), pk2(acc[i][t][2], acc[i][t][3])};
      *(__attribute__((address_space(3))) u32x2*)(smem + owbase + 2 * t * HOUT_PLANE + i * 256) = pk;
    }
  asm volatile("s_waitcnt vmcnt(0)" : "+v"(xfr[0]), "+v"(xfr[1]), "+v"(xfr[2]), "+v"(xfr[3]) :: "memory");
  f32x4 o2[4][2];
#pragma unroll
  for (int i = 0; i < 4; ++i) {
    bf16x8 X = __builtin_bit_cast(bf16x8, xfr[i]);
    o2[i][0] = mfma(Id[0], X, zero4);
    o2[i][1] = mfma(Id[1], X, zero4);
  }
  BAR();
  STAMP(10);
  A2f[3][0] = lds_tr_frag(smem + wz2_off(3) + abase0);
  A2f[3][1] = lds_tr_frag(smem + wz2_off(3) + abase1);
  {
    bf16x8 B[4][4];
#pragma unroll
    for (int ks = 0; ks < 4; ++ks)
#pragma unroll
      for (int i = 0; i < 4; ++i)
        B[ks][i] = lds_b128(smem + HOUT_OFF + (4 * ks + q) * HOUT_PLANE + ((4 * T + i) * 16 + px) * 16);
#pragma unroll
    for (int ks = 0; ks < 4; ++ks)
#pragma unroll
      for (int i = 0; i < 4; ++i) {
        o2[i][0] = mfma(A2f[ks][0], B[ks][i], o2[i][0]);
        o2[i][1] = mfma(A2f[ks][1], B[ks][i], o2[i][1]);
      }
  }
  const int sq = ((q & 1) << 1) | (q >> 1);
#pragma unroll
  for (int i = 0; i < 4; ++i) {
    u32x4 pk = pack_chunk(o2[i][0], o2[i][1]);
    unsigned o = (unsigned)(((bimg * 128 + ty0 + 4 * T + i) * 128 + tx0 + px) * 256 + (4 * w + sq) * 16);
    __builtin_amdgcn_raw_buffer_store_b128(pk, r_o, o, 0, STORE_AUX);
  }
  STAMP(11);
}

extern "C" void kernel_launch(void* const* d_in, const int* in_sizes, int n_in,
                              void* d_out, int out_size, void* d_ws, size_t ws_size,
                              hipStream_t stream) {
  const __hip_bfloat16* xf  = (const __hip_bfloat16*)d_in[0];
  const __hip_bfloat16* x   = (const __hip_bfloat16*)d_in[1];
  const __hip_bfloat16* c   = (const __hip_bfloat16*)d_in[2];
  const __hip_bfloat16* wz1 = (const __hip_bfloat16*)d_in[3];
  const __hip_bfloat16* wz2 = (const __hip_bfloat16*)d_in[4];
  const __hip_bfloat16* wc  = (const __hip_bfloat16*)d_in[5];
  (void)hipFuncSetAttribute((const void*)controlnet_fused, hipFuncAttributeMaxDynamicSharedMemorySize, LDS_BYTES);
  controlnet_fused<<<256, 512, LDS_BYTES, stream>>>(xf, x, c, wz1, wz2, wc, (__hip_bfloat16*)d_out
  );
}
```

```cpp
#include <hip/hip_runtime.h>
#include <hip/hip_bf16.h>

typedef __attribute__((ext_vector_type(8))) __bf16 bf16x8;
typedef __attribute__((ext_vector_type(2))) __bf16 bf16x2;
typedef __attribute__((ext_vector_type(4))) short s16x4;
typedef __attribute__((ext_vector_type(8))) short s16x8;
typedef __attribute__((ext_vector_type(4))) float f32x4;
typedef __attribute__((ext_vector_type(2))) float f32x2;
typedef __attribute__((ext_vector_type(4))) unsigned u32x4;
typedef __attribute__((ext_vector_type(2))) unsigned u32x2;
typedef __attribute__((address_space(3))) char lds_char;

#define DEVINL __device__ __forceinline__

constexpr int PLANE     = 3072;
constexpr int H_OFF     = 0;
constexpr int SL0       = 49152;
constexpr int C_OFF     = 73728;
constexpr int SL2       = C_OFF, SL3 = C_OFF + 24576;
constexpr int WZ1_OFF   = 122880;
constexpr int SL1       = WZ1_OFF;
constexpr int STATS_OFF = WZ1_OFF + 32768;
constexpr int LDS_BYTES = STATS_OFF + 6144;
constexpr int HOUT_OFF  = C_OFF;
__host__ __device__ constexpr int slot_off(int s) { return (s & 3) == 0 ? SL0 : (s & 3) == 1 ? SL1 : (s & 3) == 2 ? SL2 : SL3; }
__host__ __device__ constexpr int wz2_off(int ks) { return ks < 3 ? SL0 + ks * 8192 : SL1; }
constexpr int HOUT_PLANE = 2048;
constexpr unsigned OOB  = 0x40000000u;
constexpr int TENSOR_BYTES = 2 * 128 * 128 * 128 * 2;

#define STAMP(i) do {} while (0)
#define STORE_AUX 17
#define WAIT_VM(n) asm volatile("s_waitcnt vmcnt(" #n ")" ::: "memory")
#define BAR() asm volatile("s_waitcnt lgkmcnt(0)\n\ts_barrier" ::: "memory")
#define SCHED() __builtin_amdgcn_sched_barrier(0)

template <int N> struct IC { static constexpr int value = N; };
template <int I, int N, class F> DEVINL void static_for(F&& f) { if constexpr (I < N) { f(IC<I>{}); static_for<I + 1, N>(f); } }

DEVINL u32x4 make_rsrc(const void* p, unsigned bytes) {
  unsigned long long a = (unsigned long long)p;
  u32x4 r = {(unsigned)a, (unsigned)(a >> 32) & 0xffffu, bytes, 0x00020000u};
  return r;
}
DEVINL void dma16(u32x4 rsrc, unsigned voff, unsigned soff, unsigned lds_addr) {
  asm volatile("s_mov_b32 m0, %0\n\ts_nop 0\n\tbuffer_load_dwordx4 %1, %2, %3 offen lds" :: "s"(lds_addr), "v"(voff), "s"(rsrc), "s"(soff) : "memory");
}
DEVINL void dma16_nt(u32x4 rsrc, unsigned voff, unsigned soff, unsigned lds_addr) {
  asm volatile("s_mov_b32 m0, %0\n\ts_nop 0\n\tbuffer_load_dwordx4 %1, %2, %3 offen nt lds" :: "s"(lds_addr), "v"(voff), "s"(rsrc), "s"(soff) : "memory");
}
DEVINL u32x4 ld16_nt(u32x4 rsrc, unsigned voff) {
  u32x4 r;
  asm volatile("buffer_load_dwordx4 %0, %1, %2, 0 offen nt" : "=v"(r) : "v"(voff), "s"(rsrc) : "memory");
  return r;
}
DEVINL bf16x8 lds_b128(const lds_char* p) { return *(const __attribute__((address_space(3))) bf16x8*)p; }
DEVINL bf16x8 lds_tr_frag(const lds_char* p) {
  s16x4 lo = __builtin_amdgcn_ds_read_tr16_b64_v4i16((__attribute__((address_space(3))) s16x4*)p);
  s16x4 hi = __builtin_amdgcn_ds_read_tr16_b64_v4i16((__attribute__((address_space(3))) s16x4*)(p + 1024));
  s16x8 r = __builtin_shufflevector(lo, hi, 0, 1, 2, 3, 4, 5, 6, 7);
  return __builtin_bit_cast(bf16x8, r);
}
DEVINL f32x4 mfma(bf16x8 a, bf16x8 b, f32x4 c) { return __builtin_amdgcn_mfma_f32_16x16x32_bf16(a, b, c, 0, 0, 0); }
DEVINL unsigned pk2(float lo, float hi) {
  f32x2 f = {lo, hi};
  return __builtin_bit_cast(unsigned, __builtin_convertvector(f, bf16x2));
}
DEVINL float bf_lo(unsigned u) { return __uint_as_float(u << 16); }
DEVINL float bf_hi(unsigned u) { return __uint_as_float(u & 0xffff0000u); }
DEVINL void unpack_chunk(u32x4 L, f32x4& t0, f32x4& t1) {
  auto a = __builtin_amdgcn_permlane16_swap(L[0], L[2], false, false);
  auto b = __builtin_amdgcn_permlane16_swap(L[1], L[3], false, false);
  t0[0] = bf_lo(a[0]); t0[1] = bf_hi(a[0]); t0[2] = bf_lo(b[0]); t0[3] = bf_hi(b[0]);
  t1[0] = bf_lo(a[1]); t1[1] = bf_hi(a[1]); t1[2] = bf_lo(b[1]); t1[3] = bf_hi(b[1]);
}
DEVINL u32x4 pack_chunk(f32x4 t0, f32x4 t1) {
  unsigned p00 = pk2(t0[0], t0[1]), p01 = pk2(t0[2], t0[3]);
  unsigned p10 = pk2(t1[0], t1[1]), p11 = pk2(t1[2], t1[3]);
  auto a = __builtin_amdgcn_permlane16_swap(p00, p10, false, false);
  auto b = __builtin_amdgcn_permlane16_swap(p01, p11, false, false);
  u32x4 r = {a[0], b[0], a[1], b[1]};
  return r;
}
DEVINL float reduce4(float a, float b, float c, float d) {
  auto r0 = __builtin_amdgcn_permlane16_swap(__float_as_uint(a), __float_as_uint(b), false, false);
  auto r1 = __builtin_amdgcn_permlane16_swap(__float_as_uint(c), __float_as_uint(d), false, false);
  float ab = __uint_as_float(r0[0]) + __uint_as_float(r0[1]);
  float cd = __uint_as_float(r1[0]) + __uint_as_float(r1[1]);
  auto r2 = __builtin_amdgcn_permlane32_swap(__float_as_uint(ab), __float_as_uint(cd), false, false);
  return __uint_as_float(r2[0]) + __uint_as_float(r2[1]);
}

__global__ __launch_bounds__(512, 2)
void controlnet_fused(const __hip_bfloat16* __restrict__ xf, const __hip_bfloat16* __restrict__ x,
                      const __hip_bfloat16* __restrict__ c, const __hip_bfloat16* __restrict__ wz1,
                      const __hip_bfloat16* __restrict__ wz2, const __hip_bfloat16* __restrict__ wconv,
                      __hip_bfloat16* __restrict__ out
                      ) {
  extern __shared__ __attribute__((aligned(16))) char smem_generic[];
  lds_char* smem = (lds_char*)smem_generic;
  const unsigned lds0 = (unsigned)(size_t)smem;

  const int tid = threadIdx.x;
  const int lane = tid & 63;
  const int wv = __builtin_amdgcn_readfirstlane(tid >> 6);
  const int T = wv >> 2, w = wv & 3;
  const int q = lane >> 4, px = lane & 15;
  STAMP(0);

  const int bid = blockIdx.x;
  const int tile = (bid & 7) * 32 + (bid >> 3);
  const int bimg = tile >> 7, ty0 = ((tile >> 3) & 15) * 8, tx0 = (tile & 7) * 16;

  const u32x4 r_c  = make_rsrc(c, TENSOR_BYTES), r_x = make_rsrc(x, TENSOR_BYTES), r_xf = make_rsrc(xf, TENSOR_BYTES);
  const u32x4 r_w1 = make_rsrc(wz1, 32768), r_w2 = make_rsrc(wz2, 32768), r_wc = make_rsrc(wconv, 9 * 32768);
  __amdgpu_buffer_rsrc_t r_o  = __builtin_amdgcn_make_buffer_rsrc((void*)out, 0, TENSOR_BYTES, 0x00020000);

  auto pix_off = [&](int r, int cc) -> unsigned {
    int gy = ty0 - 1 + r, gx = tx0 - 1 + cc;
    bool ok = ((unsigned)gy < 128u) && ((unsigned)gx < 128u);
    return ok ? (unsigned)(((bimg * 128 + gy) * 128 + gx) * 256) : OOB;
  };
  auto grp_rc = [&](int j, int p, int& r, int& cc, bool& ok) {
    if (j < 5) { r = (T == 0) ? j : (j == 0 ? 9 : j + 4); cc = p + 1; ok = true; }
    else { r = 5 * T + (p >> 1); cc = (p & 1) ? 17 : 0; ok = p < 10; }
  };

  const int wk = 4 * wv + q;
  const int wf = (wk & 3) | (((wk >> 3) & 1) << 2);
  const unsigned wvoff = (unsigned)(wk * 256 + ((px ^ (wf << 1)) * 16));
  const unsigned wlds = lds0 + wv * 1024;
  auto dma_stage = [&](int s) {
    const int dj = s >> 2, ks = s & 3;
#pragma unroll
    for (int di = 0; di < 3; ++di)
      dma16(r_wc, wvoff, (di * 3 + dj) * 32768 + ks * 8192, wlds + slot_off(s) + di * 8192);
  };

  unsigned warm0, warm1;
  {
    const int sl = bid >> 3;
    unsigned o = tid < 72 ? (unsigned)(sl * 9216 + tid * 128) : OOB;
    unsigned o2 = tid < 8 ? (unsigned)(sl * 1024 + tid * 128) : OOB;
    asm volatile("buffer_load_dword %0, %1, %2, 0 offen" : "=v"(warm0) : "v"(o), "s"(r_wc) : "memory");
    asm volatile("buffer_load_dword %0, %1, %2, 0 offen" : "=v"(warm1) : "v"(o2), "s"(r_w2) : "memory");
  }
  unsigned warm2, warm3;
  {
    unsigned o = tid == 0 ? (unsigned)(((bimg * 128 + ty0) * 128 + tx0) * 256) : OOB;
    const u32x4 r_oo = make_rsrc(out, TENSOR_BYTES);
    asm volatile("buffer_load_dword %0, %1, %2, 0 offen" : "=v"(warm2) : "v"(o), "s"(r_oo) : "memory");
    asm volatile("buffer_load_dword %0, %1, %2, 0 offen" : "=v"(warm3) : "v"(o), "s"(r_xf) : "memory");
  }
#pragma unroll
  for (int ks = 0; ks < 4; ++ks) dma16(r_w1, wvoff, ks * 8192, wlds + WZ1_OFF + ks * 8192);
  u32x4 xr[6];
  int hpix[6];
#pragma unroll
  for (int k = 0; k < 3; ++k) {
#pragma unroll
    for (int jj = 0; jj < 2; ++jj) {
      const int j = 2 * k + jj;
      int r, cc; bool ok; const int p = 4 * w + q;
      grp_rc(j, p, r, cc, ok);
      unsigned o = ok ? pix_off(r, cc) : OOB;
      o += (unsigned)((px ^ p) * 16);
      dma16_nt(r_c, o, 0, lds0 + C_OFF + ((k * 4 + T * 2 + jj) * 16 + 4 * w) * 256);
    }
#pragma unroll
    for (int jj = 0; jj < 2; ++jj) {
      const int j = 2 * k + jj;
      int r, cc; bool ok;
      grp_rc(j, px, r, cc, ok);
      unsigned o = ok ? pix_off(r, cc) : OOB;
      hpix[j] = ok ? r * 18 + cc : 180 + 6 * T + (px - 10);
      xr[j] = ld16_nt(r_x, o + (unsigned)((4 * w + q) * 16));
    }
  }
  dma_stage(0);

  const int aq = (lane & 15) >> 2, ap = lane & 3;
  const int af = aq | ((q & 1) << 2);
  const int abase0 = (8 * q + aq) * 256 + (((2 * w) ^ af) * 32) + 8 * ap;
  const int abase1 = abase0 ^ 32;
  bf16x8 Id[2];
  {
    const int jj = px & 7;
    const unsigned one = (jj & 1) ? 0x3F800000u : 0x00003F80u;
    const bool on = ((px >> 3) == (q & 1));
    u32x4 v;
#pragma unroll
    for (int d = 0; d < 4; ++d) v[d] = (on && (jj >> 1) == d) ? one : 0u;
    u32x4 z = {0u, 0u, 0u, 0u};
    Id[0] = __builtin_bit_cast(bf16x8, (q >> 1) == 0 ? v : z);
    Id[1] = __builtin_bit_cast(bf16x8, (q >> 1) == 1 ? v : z);
  }
  const f32x4 zero4 = {0.f, 0.f, 0.f, 0.f};
  const int hwbase = H_OFF + (4 * w + (q >> 1)) * PLANE + (q & 1) * 8;

  f32x4 hin[6][2];
  bf16x8 A1f[4][2];
  bf16x8 Af[2][3][2], Bf[2][6];
  u32x4 xfr[4];
  static_for<0, 3>([&](auto kc) __attribute__((always_inline)) {
    constexpr int k = decltype(kc)::value;
    if (k == 0) asm volatile("s_waitcnt vmcnt(11)" : "+v"(xr[0]), "+v"(xr[1]), "+v"(warm0), "+v"(warm1), "+v"(warm2), "+v"(warm3) :: "memory");
    if (k == 1) asm volatile("s_waitcnt vmcnt(10)" : "+v"(xr[2]), "+v"(xr[3]) :: "memory");
    if (k == 2) asm volatile("s_waitcnt vmcnt(9)" : "+v"(xr[4]), "+v"(xr[5]) :: "memory");
    BAR();
    STAMP(1 + k);
    if (k == 0) {
#pragma unroll
      for (int ks = 0; ks < 4; ++ks) {
        A1f[ks][0] = lds_tr_frag(smem + WZ1_OFF + ks * 8192 + abase0);
        A1f[ks][1] = lds_tr_frag(smem + WZ1_OFF + ks * 8192 + abase1);
      }
    }
    float st[4];
#pragma unroll
    for (int jj = 0; jj < 2; ++jj) {
      const int j = 2 * k + jj;
      bf16x8 B[4];
#pragma unroll
      for (int ks = 0; ks < 4; ++ks)
        B[ks] = lds_b128(smem + C_OFF + ((k * 4 + T * 2 + jj) * 16 + px) * 256 + (((4 * ks + q) ^ px) * 16));
      bf16x8 X = __builtin_bit_cast(bf16x8, xr[j]);
      hin[j][0] = mfma(Id[0], X, zero4);
      hin[j][1] = mfma(Id[1], X, zero4);
#pragma unroll
      for (int ks = 0; ks < 4; ++ks) {
        hin[j][0] = mfma(A1f[ks][0], B[ks], hin[j][0]);
        hin[j][1] = mfma(A1f[ks][1], B[ks], hin[j][1]);
      }
    }
#pragma unroll
    for (int jj = 0; jj < 2; ++jj) {
      const int j = 2 * k + jj;
      float a0 = hin[j][0][0] + hin[j][0][1], a1 = hin[j][0][2] + hin[j][0][3];
      float a2 = hin[j][1][0] + hin[j][1][1], a3 = hin[j][1][2] + hin[j][1][3];
      float b0 = hin[j][0][0] * hin[j][0][0], b1 = hin[j][0][2] * hin[j][0][2];
      float b2 = hin[j][1][0] * hin[j][1][0], b3 = hin[j][1][2] * hin[j][1][2];
      b0 = fmaf(hin[j][0][1], hin[j][0][1], b0); b1 = fmaf(hin[j][0][3], hin[j][0][3], b1);
      b2 = fmaf(hin[j][1][1], hin[j][1][1], b2); b3 = fmaf(hin[j][1][3], hin[j][1][3], b3);
      st[2 * jj] = (a0 + a1) + (a2 + a3);
      st[2 * jj + 1] = (b0 + b1) + (b2 + b3);
    }
    {
      float r = reduce4(st[0], st[1], st[2], st[3]);
      const int g = 2 * k + (q >> 1);
      *(__attribute__((address_space(3))) float*)(smem + STATS_OFF + (w >> 1) * 3072 + ((T * 6 + g) * 16 + px) * 16 + (w & 1) * 8 + (q & 1) * 4) = r;
    }
    if (k == 2) WAIT_VM(6);
    BAR();
    STAMP(4 + k);
    f32x4 sp0[2], sp1[2];
#pragma unroll
    for (int jj = 0; jj < 2; ++jj) {
      const lds_char* sp = smem + STATS_OFF + ((T * 6 + 2 * k + jj) * 16 + px) * 16;
      sp0[jj] = *(const __attribute__((address_space(3))) f32x4*)(sp);
      sp1[jj] = *(const __attribute__((address_space(3))) f32x4*)(sp + 3072);
    }
    if (k == 2) {
#pragma unroll
      for (int i = 0; i < 4; ++i) {
        unsigned o = (unsigned)(((bimg * 128 + ty0 + 4 * T + i) * 128 + tx0 + px) * 256 + (4 * w + q) * 16);
        xfr[i] = ld16_nt(r_xf, o);
      }
    }
    dma_stage(k + 1);
    if (k == 2) {
#pragma unroll
      for (int di = 0; di < 3; ++di) {
        Af[0][di][0] = lds_tr_frag(smem + SL0 + di * 8192 + abase0);
        Af[0][di][1] = lds_tr_frag(smem + SL0 + di * 8192 + abase1);
      }
    }
    float rs[2], nb[2];
#pragma unroll
    for (int jj = 0; jj < 2; ++jj) {
      float sum = (sp0[jj][0] + sp0[jj][2]) + (sp1[jj][0] + sp1[jj][2]);
      float sq2 = (sp0[jj][1] + sp0[jj][3]) + (sp1[jj][1] + sp1[jj][3]);
      float mean = sum * (1.f / 128.f);
      float var = fmaxf(fmaf(sq2, 1.f / 128.f, -mean * mean), 0.f);
      rs[jj] = __builtin_amdgcn_rsqf(var + 1e-5f);
      nb[jj] = -mean * rs[jj];
    }
    float y[16], d[16];
#pragma unroll
    for (int v = 0; v < 16; ++v) y[v] = fmaf(hin[2 * k + (v >> 3)][(v >> 2) & 1][v & 3], rs[v >> 3], nb[v >> 3]);
#pragma unroll
    for (int v = 0; v < 16; ++v) d[v] = y[v] * -1.4426950408889634f;
#pragma unroll
    for (int v = 0; v < 16; ++v) d[v] = __builtin_amdgcn_exp2f(d[v]);
#pragma unroll
    for (int v = 0; v < 16; ++v) d[v] += 1.f;
#pragma unroll
    for (int g4 = 0; g4 < 4; ++g4) {
      const int v = 4 * g4;
      float p01 = d[v] * d[v + 1], p23 = d[v + 2] * d[v + 3];
      float rr = __builtin_amdgcn_rcpf(p01 * p23);
      float r01 = rr * p23, r23 = rr * p01;
      y[v] *= d[v + 1]; y[v + 1] *= d[v]; y[v + 2] *= d[v + 3]; y[v + 3] *= d[v + 2];
      y[v] *= r01; y[v + 1] *= r01; y[v + 2] *= r23; y[v + 3] *= r23;
    }
#pragma unroll
    for (int jj = 0; jj < 2; ++jj)
#pragma unroll
      for (int t = 0; t < 2; ++t) {
        const int v = 8 * jj + 4 * t;
        u32x2 pk = {pk2(y[v], y[v + 1]), pk2(y[v + 2], y[v + 3])};
        *(__attribute__((address_space(3))) u32x2*)(smem + hwbase + 2 * t * PLANE + hpix[2 * k + jj] * 16) = pk;
      }
  });
  f32x4 acc[4][2];
#pragma unroll
  for (int i = 0; i < 4; ++i) { acc[i][0] = hin[i + 1][0]; acc[i][1] = hin[i + 1][1]; }

  STAMP(7);
  WAIT_VM(10);
  BAR();
  STAMP(8);

  const lds_char* hb = smem + H_OFF + q * PLANE + (T * 4 * 18 + px) * 16;
#pragma unroll
  for (int rr = 0; rr < 6; ++rr) Bf[0][rr] = lds_b128(hb + rr * 18 * 16);
  bf16x8 A2f[4][2];
  static_for<0, 12>([&](auto sc) __attribute__((always_inline)) {
    constexpr int s = decltype(sc)::value;
    constexpr int cur = s & 1, nxt = cur ^ 1;
    SCHED();
    if constexpr (s > 0) {
      if constexpr (s == 1) WAIT_VM(10); else if constexpr (s <= 9) WAIT_VM(6); else if constexpr (s == 10) WAIT_VM(4); else WAIT_VM(1);
      BAR();
    }
    if constexpr (s + 4 < 12) dma_stage(s + 4);
    else if constexpr (s == 8) { dma16(r_w2, wvoff, 0, wlds + wz2_off(0)); dma16(r_w2, wvoff, 8192, wlds + wz2_off(1)); dma16(r_w2, wvoff, 16384, wlds + wz2_off(2)); }
    else if constexpr (s == 9) { dma16(r_w2, wvoff, 24576, wlds + wz2_off(3)); }
    SCHED();
    if constexpr (s + 1 < 12) {
      constexpr int s1 = s + 1, dj = s1 >> 2, ks = s1 & 3;
#pragma unroll
      for (int di = 0; di < 3; ++di) {
        Af[nxt][di][0] = lds_tr_frag(smem + slot_off(s1) + di * 8192 + abase0);
        Af[nxt][di][1] = lds_tr_frag(smem + slot_off(s1) + di * 8192 + abase1);
      }
#pragma unroll
      for (int rr = 0; rr < 6; ++rr) Bf[nxt][rr] = lds_b128(hb + ks * 4 * PLANE + (rr * 18 + dj) * 16);
    } else {
#pragma unroll
      for (int ks = 0; ks < 3; ++ks) {
        A2f[ks][0] = lds_tr_frag(smem + wz2_off(ks) + abase0);
        A2f[ks][1] = lds_tr_frag(smem + wz2_off(ks) + abase1);
      }
    }
#pragma unroll
    for (int di = 0; di < 3; ++di)
#pragma unroll
      for (int i = 0; i < 4; ++i) {
        acc[i][0] = mfma(Af[cur][di][0], Bf[cur][i + di], acc[i][0]);
        acc[i][1] = mfma(Af[cur][di][1], Bf[cur][i + di], acc[i][1]);
      }
    if constexpr (s + 1 < 12) {
#pragma unroll
      for (int g = 0; g < 18; ++g) {
        __builtin_amdgcn_sched_group_barrier(0x008, 1, 0);
        __builtin_amdgcn_sched_group_barrier(0x100, 1, 0);
      }
      __builtin_amdgcn_sched_group_barrier(0x008, 6, 0);
    } else {
#pragma unroll
      for (int g = 0; g < 12; ++g) {
        __builtin_amdgcn_sched_group_barrier(0x008, 1, 0);
        __builtin_amdgcn_sched_group_barrier(0x100, 1, 0);
      }
      __builtin_amdgcn_sched_group_barrier(0x008, 12, 0);
    }
  });
  SCHED();
  STAMP(9);
  const int owbase = HOUT_OFF + (4 * w + (q >> 1)) * HOUT_PLANE + (q & 1) * 8 + (4 * T * 16 + px) * 16;
#pragma unroll
  for (int i = 0; i < 4; ++i)
#pragma unroll
    for (int t = 0; t < 2; ++t) {
      u32x2 pk = {pk2(acc[i][t][0], acc[i][t][1]), pk2(acc[i][t][2], acc[i][t][3])};
      *(__attribute__((address_space(3))) u32x2*)(smem + owbase + 2 * t * HOUT_PLANE + i * 256) = pk;
    }
  asm volatile("s_waitcnt vmcnt(0)" : "+v"(xfr[0]), "+v"(xfr[1]), "+v"(xfr[2]), "+v"(xfr[3]) :: "memory");
  f32x4 o2[4][2];
#pragma unroll
  for (int i = 0; i < 4; ++i) {
    bf16x8 X = __builtin_bit_cast(bf16x8, xfr[i]);
    o2[i][0] = mfma(Id[0], X, zero4);
    o2[i][1] = mfma(Id[1], X, zero4);
  }
  BAR();
  STAMP(10);
  A2f[3][0] = lds_tr_frag(smem + wz2_off(3) + abase0);
  A2f[3][1] = lds_tr_frag(smem + wz2_off(3) + abase1);
  {
    bf16x8 B[4][4];
#pragma unroll
    for (int ks = 0; ks < 4; ++ks)
#pragma unroll
      for (int i = 0; i < 4; ++i)
        B[ks][i] = lds_b128(smem + HOUT_OFF + (4 * ks + q) * HOUT_PLANE + ((4 * T + i) * 16 + px) * 16);
#pragma unroll
    for (int ks = 0; ks < 4; ++ks)
#pragma unroll
      for (int i = 0; i < 4; ++i) {
        o2[i][0] = mfma(A2f[ks][0], B[ks][i], o2[i][0]);
        o2[i][1] = mfma(A2f[ks][1], B[ks][i], o2[i][1]);
      }
  }
  const int sq = ((q & 1) << 1) | (q >> 1);
#pragma unroll
  for (int i = 0; i < 4; ++i) {
    u32x4 pk = pack_chunk(o2[i][0], o2[i][1]);
    unsigned o = (unsigned)(((bimg * 128 + ty0 + 4 * T + i) * 128 + tx0 + px) * 256 + (4 * w + sq) * 16);
    __builtin_amdgcn_raw_buffer_store_b128(pk, r_o, o, 0, STORE_AUX);
  }
  STAMP(11);
}

extern "C" void kernel_launch(void* const* d_in, const int* in_sizes, int n_in,
                              void* d_out, int out_size, void* d_ws, size_t ws_size,
                              hipStream_t stream) {
  const __hip_bfloat16* xf  = (const __hip_bfloat16*)d_in[0];
  const __hip_bfloat16* x   = (const __hip_bfloat16*)d_in[1];
  const __hip_bfloat16* c   = (const __hip_bfloat16*)d_in[2];
  const __hip_bfloat16* wz1 = (const __hip_bfloat16*)d_in[3];
  const __hip_bfloat16* wz2 = (const __hip_bfloat16*)d_in[4];
  const __hip_bfloat16* wc  = (const __hip_bfloat16*)d_in[5];
  (void)hipFuncSetAttribute((const void*)controlnet_fused, hipFuncAttributeMaxDynamicSharedMemorySize, LDS_BYTES);
  controlnet_fused<<<256, 512, LDS_BYTES, stream>>>(xf, x, c, wz1, wz2, wc, (__hip_bfloat16*)d_out
  );
}
```

```cpp
#include <hip/hip_runtime.h>
#include <hip/hip_bf16.h>

typedef __attribute__((ext_vector_type(8))) __bf16 bf16x8;
typedef __attribute__((ext_vector_type(2))) __bf16 bf16x2;
typedef __attribute__((ext_vector_type(4))) short s16x4;
typedef __attribute__((ext_vector_type(8))) short s16x8;
typedef __attribute__((ext_vector_type(4))) float f32x4;
typedef __attribute__((ext_vector_type(2))) float f32x2;
typedef __attribute__((ext_vector_type(4))) unsigned u32x4;
typedef __attribute__((ext_vector_type(2))) unsigned u32x2;
typedef __attribute__((address_space(3))) char lds_char;
#define LDSP(T, p) ((__attribute__((address_space(3))) T*)(p))
#define DEVINL __device__ __forceinline__
#define LAMBDA __attribute__((always_inline))

constexpr int PLANE     = 3072;
constexpr int H_OFF     = 0;
constexpr int R0 = 49152, R1 = 81920, R2 = 114688;
constexpr int STATS_OFF = 147456;
constexpr int LDS_BYTES = STATS_OFF + 6144;
constexpr int HOUT_OFF  = R2;
constexpr int HOUT_PLANE = 2048;
__host__ __device__ constexpr int reg_off(int a) { return a == 0 ? R0 : a == 1 ? R1 : R2; }
__host__ __device__ constexpr int blk_off(int s, int di) { return (s & 3) < 3 ? reg_off(s & 3) + di * 8192 : reg_off(di) + 24576; }
__host__ __device__ constexpr int wz2_off(int ks) { return ks < 3 ? R0 + ks * 8192 : R1; }
constexpr unsigned OOB  = 0x40000000u;
constexpr int TENSOR_BYTES = 2 * 128 * 128 * 128 * 2;
constexpr int NTHREADS = 576;

#define STAMP(i) do {} while (0)
#define STORE_AUX 17
#define WAIT_VM(n) asm volatile("s_waitcnt vmcnt(" #n ")" ::: "memory")
#define BAR() asm volatile("s_waitcnt lgkmcnt(0)\n\ts_barrier" ::: "memory")
#define SCHED() __builtin_amdgcn_sched_barrier(0)

template <int N> struct IC { static constexpr int value = N; };
template <int I, int N, class F> DEVINL void static_for(F&& f) { if constexpr (I < N) { f(IC<I>{}); static_for<I + 1, N>(f); } }

DEVINL u32x4 make_rsrc(const void* p, unsigned bytes) {
  unsigned long long a = (unsigned long long)p;
  u32x4 r = {(unsigned)a, (unsigned)(a >> 32) & 0xffffu, bytes, 0x00020000u};
  return r;
}
DEVINL void dma16(u32x4 rsrc, unsigned voff, unsigned soff, unsigned lds_addr) {
  asm volatile("s_mov_b32 m0, %0\n\ts_nop 0\n\tbuffer_load_dwordx4 %1, %2, %3 offen lds" :: "s"(lds_addr), "v"(voff), "s"(rsrc), "s"(soff) : "memory");
}
DEVINL void dma16_nt(u32x4 rsrc, unsigned voff, unsigned lds_addr) {
  asm volatile("s_mov_b32 m0, %0\n\ts_nop 0\n\tbuffer_load_dwordx4 %1, %2, 0 offen nt lds" :: "s"(lds_addr), "v"(voff), "s"(rsrc) : "memory");
}
DEVINL u32x4 ld16_nt(u32x4 rsrc, unsigned voff) {
  u32x4 r;
  asm volatile("buffer_load_dwordx4 %0, %1, %2, 0 offen nt" : "=v"(r) : "v"(voff), "s"(rsrc) : "memory");
  return r;
}
DEVINL bf16x8 lds_b128(const lds_char* p) { return *LDSP(const bf16x8, p); }
DEVINL bf16x8 lds_tr_frag(const lds_char* p) {
  s16x4 lo = __builtin_amdgcn_ds_read_tr16_b64_v4i16(LDSP(s16x4, p));
  s16x4 hi = __builtin_amdgcn_ds_read_tr16_b64_v4i16(LDSP(s16x4, p + 1024));
  s16x8 r = __builtin_shufflevector(lo, hi, 0, 1, 2, 3, 4, 5, 6, 7);
  return __builtin_bit_cast(bf16x8, r);
}
DEVINL f32x4 mfma(bf16x8 a, bf16x8 b, f32x4 c) { return __builtin_amdgcn_mfma_f32_16x16x32_bf16(a, b, c, 0, 0, 0); }
DEVINL unsigned pk2(float lo, float hi) {
  f32x2 f = {lo, hi};
  return __builtin_bit_cast(unsigned, __builtin_convertvector(f, bf16x2));
}
DEVINL unsigned pk2v(f32x2 f) { return __builtin_bit_cast(unsigned, __builtin_convertvector(f, bf16x2)); }
DEVINL float bf_lo(unsigned u) { return __builtin_bit_cast(float, u << 16); }
DEVINL float bf_hi(unsigned u) { return __builtin_bit_cast(float, u & 0xffff0000u); }
DEVINL unsigned f2u(float f) { return __builtin_bit_cast(unsigned, f); }
DEVINL float u2f(unsigned u) { return __builtin_bit_cast(float, u); }
DEVINL f32x4 unpack4(u32x2 v) { f32x4 r = {bf_lo(v[0]), bf_hi(v[0]), bf_lo(v[1]), bf_hi(v[1])}; return r; }
DEVINL f32x2 lo2(f32x4 v) { return __builtin_shufflevector(v, v, 0, 1); }
DEVINL f32x2 hi2(f32x4 v) { return __builtin_shufflevector(v, v, 2, 3); }
DEVINL f32x2 bc2(float v) { f32x2 r = {v, v}; return r; }
DEVINL f32x2 fma2(f32x2 a, f32x2 b, f32x2 c) { return __builtin_elementwise_fma(a, b, c); }
DEVINL void unpack_chunk(u32x4 L, f32x4& t0, f32x4& t1) {
  auto a = __builtin_amdgcn_permlane16_swap(L[0], L[2], false, false);
  auto b = __builtin_amdgcn_permlane16_swap(L[1], L[3], false, false);
  t0[0] = bf_lo(a[0]); t0[1] = bf_hi(a[0]); t0[2] = bf_lo(b[0]); t0[3] = bf_hi(b[0]);
  t1[0] = bf_lo(a[1]); t1[1] = bf_hi(a[1]); t1[2] = bf_lo(b[1]); t1[3] = bf_hi(b[1]);
}
DEVINL u32x4 pack_chunk(f32x4 t0, f32x4 t1) {
  unsigned p00 = pk2(t0[0], t0[1]), p01 = pk2(t0[2], t0[3]);
  unsigned p10 = pk2(t1[0], t1[1]), p11 = pk2(t1[2], t1[3]);
  auto a = __builtin_amdgcn_permlane16_swap(p00, p10, false, false);
  auto b = __builtin_amdgcn_permlane16_swap(p01, p11, false, false);
  u32x4 r = {a[0], b[0], a[1], b[1]};
  return r;
}
DEVINL float reduce4(float a, float b, float c, float d) {
  auto r0 = __builtin_amdgcn_permlane16_swap(f2u(a), f2u(b), false, false);
  auto r1 = __builtin_amdgcn_permlane16_swap(f2u(c), f2u(d), false, false);
  float ab = u2f(r0[0]) + u2f(r0[1]);
  float cd = u2f(r1[0]) + u2f(r1[1]);
  auto r2 = __builtin_amdgcn_permlane32_swap(f2u(ab), f2u(cd), false, false);
  return u2f(r2[0]) + u2f(r2[1]);
}

__global__ __launch_bounds__(NTHREADS, 3)
void controlnet_fused(const __hip_bfloat16* __restrict__ xf, const __hip_bfloat16* __restrict__ x,
                      const __hip_bfloat16* __restrict__ c, const __hip_bfloat16* __restrict__ wz1,
                      const __hip_bfloat16* __restrict__ wz2, const __hip_bfloat16* __restrict__ wconv,
                      __hip_bfloat16* __restrict__ out
                      ) {
  extern __shared__ __attribute__((aligned(16))) char smem_generic[];
  lds_char* smem = (lds_char*)smem_generic;
  const unsigned lds0 = (unsigned)(size_t)smem;

  const int tid = threadIdx.x;
  const int lane = tid & 63;
  const int wv = __builtin_amdgcn_readfirstlane(tid >> 6);
  const int q = lane >> 4, px = lane & 15;
  const int bid = blockIdx.x;

  if (wv == 8) {
    const u32x4 r_w2 = make_rsrc(wz2, 32768), r_wc = make_rsrc(wconv, 9 * 32768);
    const unsigned vA = (unsigned)(q * 256 + ((px ^ (q << 1)) * 16));
    const unsigned vB = (unsigned)(q * 256 + ((px ^ ((q | 4) << 1)) * 16));
    auto block = [&](u32x4 rs, int src, int dst) LAMBDA {
#pragma unroll
      for (int p = 0; p < 8; ++p) dma16(rs, ((p >> 1) & 1) ? vB : vA, src + p * 1024, lds0 + dst + p * 1024);
    };
    auto stage = [&](int s) LAMBDA {
#pragma unroll
      for (int di = 0; di < 3; ++di) block(r_wc, (di * 3 + (s >> 2)) * 32768 + (s & 3) * 8192, blk_off(s, di));
    };
    BAR();
    BAR();
    stage(0);
    BAR();
    stage(1);
    WAIT_VM(24); BAR();
    stage(2); stage(3);
    WAIT_VM(48); BAR();
    static_for<0, 11>([&](auto sc) LAMBDA {
      constexpr int s = decltype(sc)::value;
      if constexpr (s + 4 < 12) stage(s + 4);
      else if constexpr (s == 8) { block(r_w2, 0, wz2_off(0)); block(r_w2, 8192, wz2_off(1)); block(r_w2, 16384, wz2_off(2)); }
      else if constexpr (s == 9) { block(r_w2, 24576, wz2_off(3)); }
      if constexpr (s <= 8) WAIT_VM(48); else if constexpr (s == 9) WAIT_VM(32); else WAIT_VM(8);
      BAR();
    });
    WAIT_VM(0);
    BAR();
    return;
  }

  const int T = wv >> 2, w = wv & 3;
  STAMP(0);
  const int tile = (bid & 7) * 32 + (bid >> 3);
  const int bimg = tile >> 7, ty0 = ((tile >> 3) & 15) * 8, tx0 = (tile & 7) * 16;

  const u32x4 r_c  = make_rsrc(c, TENSOR_BYTES), r_x = make_rsrc(x, TENSOR_BYTES), r_xf = make_rsrc(xf, TENSOR_BYTES);
  const u32x4 r_w1 = make_rsrc(wz1, 32768);
  __amdgpu_buffer_rsrc_t r_o  = __builtin_amdgcn_make_buffer_rsrc((void*)out, 0, TENSOR_BYTES, 0x00020000);
  {
    const int wk = 4 * wv + q;
    const int wf = (wk & 3) | (((wk >> 3) & 1) << 2);
    const unsigned wvoff = (unsigned)(wk * 256 + ((px ^ (wf << 1)) * 16));
#pragma unroll
    for (int ks = 0; ks < 4; ++ks) dma16(r_w1, wvoff, ks * 8192, lds0 + wv * 1024 + H_OFF + ks * 8192);
  }
  auto pix_off = [&](int r, int cc) LAMBDA -> unsigned {
    int gy = ty0 - 1 + r, gx = tx0 - 1 + cc;
    bool ok = ((unsigned)gy < 128u) && ((unsigned)gx < 128u);
    return ok ? (unsigned)(((bimg * 128 + gy) * 128 + gx) * 256) : OOB;
  };
  auto grp_rc = [&](int j, int p, int& r, int& cc, bool& ok) LAMBDA {
    if (j < 5) { r = (T == 0) ? j : (j == 0 ? 9 : j + 4); cc = p + 1; ok = true; }
    else { r = 5 * T + (p >> 1); cc = (p & 1) ? 17 : 0; ok = p < 10; }
  };
#pragma unroll
  for (int j = 0; j < 6; ++j) {
    const int k = j >> 1, jj = j & 1;
    int r, cc; bool ok; const int p = 4 * w + q;
    grp_rc(j, p, r, cc, ok);
    unsigned o = ok ? pix_off(r, cc) : OOB;
    o += (unsigned)((px ^ p) * 16);
    const unsigned dst = lds0 + reg_off(k) + (T * 2 + jj) * 8192 + w * 1024;
    dma16_nt(r_c, o, dst);
    dma16_nt(r_x, o, dst + 4096);
  }
  STAMP(13);
  int hpix[6];
#pragma unroll
  for (int j = 0; j < 6; ++j) {
    int r, cc; bool ok;
    grp_rc(j, px, r, cc, ok);
    hpix[j] = (ok ? r * 18 + cc : 180 + 6 * T + (px - 10)) * 16;
  }
  const int aq = (lane & 15) >> 2, ap = lane & 3;
  const int af = aq | ((q & 1) << 2);
  const int abase0 = (8 * q + aq) * 256 + (((2 * w) ^ af) * 32) + 8 * ap;
  const int abase1 = abase0 ^ 32;
  const int hwbase = H_OFF + (4 * w + (q >> 1)) * PLANE + (q & 1) * 8;
  int cb[4];
#pragma unroll
  for (int ks = 0; ks < 4; ++ks) cb[ks] = T * 16384 + px * 256 + (((4 * ks + q) ^ px) * 16);
  const int xb0 = T * 16384 + 4096 + px * 256 + (((4 * w + (q >> 1)) ^ px) * 16) + (q & 1) * 8;
  const int xb1 = T * 16384 + 4096 + px * 256 + (((4 * w + 2 + (q >> 1)) ^ px) * 16) + (q & 1) * 8;
  const int stw = STATS_OFF + (w >> 1) * 3072 + ((T * 6 + (q >> 1)) * 16 + px) * 16 + (w & 1) * 8 + (q & 1) * 4;
  const int str = STATS_OFF + (T * 6 * 16 + px) * 16;

  f32x4 hin[6][2];
  bf16x8 A1f[4][2];
  bf16x8 Af[2][3][2], Bf[2][6];
  u32x4 xfr[4];
  bf16x8 Cb[2][4]; u32x2 Xb[2][2];
  auto z1_read = [&](auto kc) LAMBDA {
    constexpr int k = decltype(kc)::value;
#pragma unroll
    for (int jj = 0; jj < 2; ++jj) {
      Xb[jj][0] = *LDSP(const u32x2, smem + reg_off(k) + jj * 8192 + xb0);
      Xb[jj][1] = *LDSP(const u32x2, smem + reg_off(k) + jj * 8192 + xb1);
    }
#pragma unroll
    for (int ks = 0; ks < 4; ++ks)
#pragma unroll
      for (int jj = 0; jj < 2; ++jj) Cb[jj][ks] = lds_b128(smem + reg_off(k) + jj * 8192 + cb[ks]);
  };
  auto z1_unpack = [&](auto kc) LAMBDA {
    constexpr int k = decltype(kc)::value;
#pragma unroll
    for (int jj = 0; jj < 2; ++jj) {
      hin[2 * k + jj][0] = unpack4(Xb[jj][0]);
      hin[2 * k + jj][1] = unpack4(Xb[jj][1]);
    }
  };
  auto z1_mma1 = [&](auto kc, auto ic) LAMBDA {
    constexpr int k = decltype(kc)::value, i = decltype(ic)::value;
    constexpr int ks = i >> 2, jj = (i >> 1) & 1, t = i & 1;
    hin[2 * k + jj][t] = mfma(A1f[ks][t], Cb[jj][ks], hin[2 * k + jj][t]);
  };
  auto z1_stats = [&](auto kc) LAMBDA {
    constexpr int k = decltype(kc)::value;
    float st[4];
#pragma unroll
    for (int jj = 0; jj < 2; ++jj) {
      const int j = 2 * k + jj;
      f32x2 a = (lo2(hin[j][0]) + hi2(hin[j][0])) + (lo2(hin[j][1]) + hi2(hin[j][1]));
      f32x2 b = lo2(hin[j][0]) * lo2(hin[j][0]);
      b = fma2(hi2(hin[j][0]), hi2(hin[j][0]), b);
      b = fma2(lo2(hin[j][1]), lo2(hin[j][1]), b);
      b = fma2(hi2(hin[j][1]), hi2(hin[j][1]), b);
      st[2 * jj] = a[0] + a[1];
      st[2 * jj + 1] = b[0] + b[1];
    }
    float r = reduce4(st[0], st[1], st[2], st[3]);
    *LDSP(float, smem + stw + 2 * k * 256) = r;
  };
  f32x4 sp0[2], sp1[2];
  auto si_read = [&](auto kc) LAMBDA {
    constexpr int k = decltype(kc)::value;
#pragma unroll
    for (int jj = 0; jj < 2; ++jj) {
      sp0[jj] = *LDSP(const f32x4, smem + str + (2 * k + jj) * 256);
      sp1[jj] = *LDSP(const f32x4, smem + str + (2 * k + jj) * 256 + 3072);
    }
  };
  auto si_math = [&](auto kc, auto hook) LAMBDA {
    constexpr int k = decltype(kc)::value;
    float rs2[2], nb2[2];
#pragma unroll
    for (int jj = 0; jj < 2; ++jj) {
      f32x2 m = ((lo2(sp0[jj]) + hi2(sp0[jj])) + (lo2(sp1[jj]) + hi2(sp1[jj]))) * bc2(1.f / 128.f);
      float var = __builtin_fmaxf(__builtin_fmaf(-m[0], m[0], m[1]), 0.f);
      rs2[jj] = __builtin_amdgcn_rsqf(var + 1e-5f) * -1.4426950408889634f;
      nb2[jj] = -m[0] * rs2[jj];
    }
    f32x2 t[8], d[8];
#pragma unroll
    for (int g = 0; g < 4; ++g) {
      t[2 * g]     = fma2(lo2(hin[2 * k + (g >> 1)][g & 1]), bc2(rs2[g >> 1]), bc2(nb2[g >> 1]));
      t[2 * g + 1] = fma2(hi2(hin[2 * k + (g >> 1)][g & 1]), bc2(rs2[g >> 1]), bc2(nb2[g >> 1]));
    }
    SCHED();
    static_for<0, 16>([&](auto ic) LAMBDA {
      constexpr int i = decltype(ic)::value;
      hook(ic);
      d[i >> 1][i & 1] = __builtin_amdgcn_exp2f(t[i >> 1][i & 1]);
      SCHED();
    });
#pragma unroll
    for (int p = 0; p < 8; ++p) d[p] = d[p] + bc2(1.f);
    f32x2 P[4]; float rr[4];
#pragma unroll
    for (int g = 0; g < 4; ++g) P[g] = d[2 * g] * d[2 * g + 1];
#pragma unroll
    for (int g = 0; g < 4; ++g) rr[g] = __builtin_amdgcn_rcpf(P[g][0] * P[g][1]);
#pragma unroll
    for (int g = 0; g < 4; ++g) { f32x2 a = t[2 * g] * d[2 * g + 1]; t[2 * g + 1] = t[2 * g + 1] * d[2 * g]; t[2 * g] = a; }
#pragma unroll
    for (int g = 0; g < 4; ++g) {
      f32x2 sw = __builtin_shufflevector(P[g], P[g], 1, 0);
      P[g] = sw * bc2(rr[g] * -0.6931471805599453f);
    }
#pragma unroll
    for (int g = 0; g < 4; ++g) {
      u32x2 pk = {pk2v(t[2 * g] * P[g]), pk2v(t[2 * g + 1] * P[g])};
      *LDSP(u32x2, smem + hwbase + 2 * (g & 1) * PLANE + hpix[2 * k + (g >> 1)]) = pk;
    }
  };

  WAIT_VM(8);
  BAR();
  STAMP(1);
#pragma unroll
  for (int ks = 0; ks < 4; ++ks) {
    A1f[ks][0] = lds_tr_frag(smem + H_OFF + ks * 8192 + abase0);
    A1f[ks][1] = lds_tr_frag(smem + H_OFF + ks * 8192 + abase1);
  }
  z1_read(IC<0>{});
  SCHED();
  z1_unpack(IC<0>{});
  static_for<0, 16>([&](auto ic) LAMBDA { z1_mma1(IC<0>{}, ic); });
  z1_stats(IC<0>{});
  SCHED();
  STAMP(2);
  WAIT_VM(4);
  BAR();
  STAMP(3);
  si_read(IC<0>{});
  z1_read(IC<1>{});
  SCHED();
  z1_unpack(IC<1>{});
  si_math(IC<0>{}, [&](auto ic) LAMBDA { z1_mma1(IC<1>{}, ic); });
  z1_stats(IC<1>{});
  SCHED();
  STAMP(4);
  WAIT_VM(0);
  BAR();
  STAMP(5);
  si_read(IC<1>{});
  z1_read(IC<2>{});
#pragma unroll
  for (int i = 0; i < 4; ++i) {
    unsigned o = (unsigned)(((bimg * 128 + ty0 + 4 * T + i) * 128 + tx0 + px) * 256 + (4 * w + (((q & 1) << 1) | (q >> 1))) * 16);
    xfr[i] = ld16_nt(r_xf, o);
  }
  SCHED();
  z1_unpack(IC<2>{});
  si_math(IC<1>{}, [&](auto ic) LAMBDA { z1_mma1(IC<2>{}, ic); });
  z1_stats(IC<2>{});
  SCHED();
  STAMP(6);
  BAR();
  STAMP(7);
  si_read(IC<2>{});
#pragma unroll
  for (int di = 0; di < 3; ++di) {
    Af[0][di][0] = lds_tr_frag(smem + blk_off(0, di) + abase0);
    Af[0][di][1] = lds_tr_frag(smem + blk_off(0, di) + abase1);
  }
  SCHED();
  si_math(IC<2>{}, [&](auto ic) LAMBDA {});
  f32x4 acc[4][2];
#pragma unroll
  for (int i = 0; i < 4; ++i) { acc[i][0] = hin[i + 1][0]; acc[i][1] = hin[i + 1][1]; }
  SCHED();
  STAMP(8);
  BAR();
  STAMP(9);

  const lds_char* hb = smem + H_OFF + q * PLANE + (T * 4 * 18 + px) * 16;
#pragma unroll
  for (int rr = 0; rr < 6; ++rr) Bf[0][rr] = lds_b128(hb + rr * 18 * 16);
  bf16x8 A2f[4][2];
  f32x4 o2[4][2];
  const int owbase = HOUT_OFF + (4 * w + (q >> 1)) * HOUT_PLANE + (q & 1) * 8 + (4 * T * 16 + px) * 16;
  static_for<0, 12>([&](auto sc) LAMBDA {
    constexpr int s = decltype(sc)::value;
    constexpr int cur = s & 1, nxt = cur ^ 1;
    SCHED();
    if constexpr (s > 0) BAR();
    if constexpr (s == 10) {
      asm volatile("s_waitcnt vmcnt(0)" : "+v"(xfr[0]), "+v"(xfr[1]), "+v"(xfr[2]), "+v"(xfr[3]) :: "memory");
    }
    SCHED();
    if constexpr (s + 1 < 12) {
      constexpr int s1 = s + 1, dj = s1 >> 2, ks = s1 & 3;
#pragma unroll
      for (int di = 0; di < 3; ++di) {
        Af[nxt][di][0] = lds_tr_frag(smem + blk_off(s1, di) + abase0);
        Af[nxt][di][1] = lds_tr_frag(smem + blk_off(s1, di) + abase1);
      }
#pragma unroll
      for (int rr = 0; rr < 6; ++rr) Bf[nxt][rr] = lds_b128(hb + ks * 4 * PLANE + (rr * 18 + dj) * 16);
    } else {
#pragma unroll
      for (int ks = 0; ks < 3; ++ks) {
        A2f[ks][0] = lds_tr_frag(smem + wz2_off(ks) + abase0);
        A2f[ks][1] = lds_tr_frag(smem + wz2_off(ks) + abase1);
      }
    }
    if constexpr (s == 10) {
#pragma unroll
      for (int i = 0; i < 4; ++i) unpack_chunk(xfr[i], o2[i][0], o2[i][1]);
    }
    if constexpr (s < 11) {
#pragma unroll
      for (int di = 0; di < 3; ++di)
#pragma unroll
        for (int i = 0; i < 4; ++i) {
          acc[i][0] = mfma(Af[cur][di][0], Bf[cur][i + di], acc[i][0]);
          acc[i][1] = mfma(Af[cur][di][1], Bf[cur][i + di], acc[i][1]);
        }
    } else {
#pragma unroll
      for (int i = 0; i < 4; ++i)
#pragma unroll
        for (int di = 0; di < 3; ++di) {
          acc[i][0] = mfma(Af[cur][di][0], Bf[cur][i + di], acc[i][0]);
          acc[i][1] = mfma(Af[cur][di][1], Bf[cur][i + di], acc[i][1]);
        }
#pragma unroll
      for (int i = 0; i < 4; ++i)
#pragma unroll
        for (int t = 0; t < 2; ++t) {
          u32x2 pk = {pk2(acc[i][t][0], acc[i][t][1]), pk2(acc[i][t][2], acc[i][t][3])};
          *LDSP(u32x2, smem + owbase + 2 * t * HOUT_PLANE + i * 256) = pk;
        }
    }
    if constexpr (s == 10) {
#pragma unroll
      for (int g = 0; g < 18; ++g) {
        __builtin_amdgcn_sched_group_barrier(0x008, 1, 0);
        __builtin_amdgcn_sched_group_barrier(0x100, 1, 0);
        __builtin_amdgcn_sched_group_barrier(0x002, 2, 0);
      }
      __builtin_amdgcn_sched_group_barrier(0x008, 6, 0);
    } else if constexpr (s + 1 < 12) {
#pragma unroll
      for (int g = 0; g < 18; ++g) {
        __builtin_amdgcn_sched_group_barrier(0x008, 1, 0);
        __builtin_amdgcn_sched_group_barrier(0x100, 1, 0);
      }
      __builtin_amdgcn_sched_group_barrier(0x008, 6, 0);
    } else {
#pragma unroll
      for (int g = 0; g < 12; ++g) {
        __builtin_amdgcn_sched_group_barrier(0x008, 1, 0);
        __builtin_amdgcn_sched_group_barrier(0x100, 1, 0);
      }
#pragma unroll
      for (int g = 0; g < 6; ++g) {
        __builtin_amdgcn_sched_group_barrier(0x008, 2, 0);
        __builtin_amdgcn_sched_group_barrier(0x002, 4, 0);
        __builtin_amdgcn_sched_group_barrier(0x200, 2, 0);
      }
    }
  });
  SCHED();
  STAMP(10);
  BAR();
  STAMP(11);
  A2f[3][0] = lds_tr_frag(smem + wz2_off(3) + abase0);
  A2f[3][1] = lds_tr_frag(smem + wz2_off(3) + abase1);
  {
    bf16x8 B[4][4];
#pragma unroll
    for (int i = 0; i < 4; ++i)
#pragma unroll
      for (int ks = 0; ks < 4; ++ks)
        B[ks][i] = lds_b128(smem + HOUT_OFF + (4 * ks + q) * HOUT_PLANE + ((4 * T + i) * 16 + px) * 16);
    SCHED();
    const int sq = ((q & 1) << 1) | (q >> 1);
#pragma unroll
    for (int i = 0; i < 4; ++i) {
#pragma unroll
      for (int ks = 0; ks < 4; ++ks) {
        o2[i][0] = mfma(A2f[ks][0], B[ks][i], o2[i][0]);
        o2[i][1] = mfma(A2f[ks][1], B[ks][i], o2[i][1]);
      }
    }
#pragma unroll
    for (int i = 0; i < 4; ++i) {
      u32x4 pk = pack_chunk(o2[i][0], o2[i][1]);
      unsigned o = (unsigned)(((bimg * 128 + ty0 + 4 * T + i) * 128 + tx0 + px) * 256 + (4 * w + sq) * 16);
      __builtin_amdgcn_raw_buffer_store_b128(pk, r_o, o, 0, STORE_AUX);
    }
  }
  STAMP(12);
}

extern "C" void kernel_launch(void* const* d_in, const int* in_sizes, int n_in,
                              void* d_out, int out_size, void* d_ws, size_t ws_size,
                              hipStream_t stream) {
  const __hip_bfloat16* xf  = (const __hip_bfloat16*)d_in[0];
  const __hip_bfloat16* x   = (const __hip_bfloat16*)d_in[1];
  const __hip_bfloat16* c   = (const __hip_bfloat16*)d_in[2];
  const __hip_bfloat16* wz1 = (const __hip_bfloat16*)d_in[3];
  const __hip_bfloat16* wz2 = (const __hip_bfloat16*)d_in[4];
  const __hip_bfloat16* wc  = (const __hip_bfloat16*)d_in[5];
  (void)hipFuncSetAttribute((const void*)controlnet_fused, hipFuncAttributeMaxDynamicSharedMemorySize, LDS_BYTES);
  controlnet_fused<<<256, NTHREADS, LDS_BYTES, stream>>>(xf, x, c, wz1, wz2, wc, (__hip_bfloat16*)d_out
  );
}
```

```cpp
#include <hip/hip_runtime.h>
#include <hip/hip_bf16.h>

typedef __attribute__((ext_vector_type(8))) __bf16 bf16x8;
typedef __attribute__((ext_vector_type(2))) __bf16 bf16x2;
typedef __attribute__((ext_vector_type(4))) short s16x4;
typedef __attribute__((ext_vector_type(8))) short s16x8;
typedef __attribute__((ext_vector_type(4))) float f32x4;
typedef __attribute__((ext_vector_type(2))) float f32x2;
typedef __attribute__((ext_vector_type(4))) unsigned u32x4;
typedef __attribute__((ext_vector_type(2))) unsigned u32x2;
typedef __attribute__((address_space(3))) char lds_char;
#define LDSP(T, p) ((__attribute__((address_space(3))) T*)(p))
#define DEVINL __device__ __forceinline__
#define LAMBDA __attribute__((always_inline))

constexpr int PLANE     = 3072;
constexpr int H_OFF     = 0;
constexpr int R0 = 49152, R1 = 81920, R2 = 114688;
constexpr int STATS_OFF = 147456;
constexpr int LDS_BYTES = STATS_OFF + 6144;
constexpr int HOUT_PLANE = 2048;
__host__ __device__ constexpr int reg_off(int a) { return a == 0 ? R0 : a == 1 ? R1 : R2; }
__host__ __device__ constexpr int blk_off(int s, int di) { return (s & 3) < 3 ? reg_off(s & 3) + di * 8192 : reg_off(di) + 24576; }
__host__ __device__ constexpr int step_dj(int s) { return s >> 2; }
__host__ __device__ constexpr int wz2_off(int ks) { return ks < 3 ? R0 + ks * 8192 : R1; }
__host__ __device__ constexpr int hout_off(int ks) { return ks < 3 ? R2 + ks * 8192 : R1 + 8192; }
constexpr unsigned OOB  = 0x40000000u;
constexpr int TENSOR_BYTES = 2 * 128 * 128 * 128 * 2;
constexpr int NTHREADS = 576;

#define STAMP(i) do {} while (0)
#define STORE_AUX 17
#define WAIT_VM(n) asm volatile("s_waitcnt vmcnt(" #n ")" ::: "memory")
#define BAR() asm volatile("s_waitcnt lgkmcnt(0)\n\ts_barrier" ::: "memory")
#define SCHED() __builtin_amdgcn_sched_barrier(0)

template <int N> struct IC { static constexpr int value = N; };
template <int I, int N, class F> DEVINL void static_for(F&& f) { if constexpr (I < N) { f(IC<I>{}); static_for<I + 1, N>(f); } }

DEVINL u32x4 make_rsrc(const void* p, unsigned bytes) {
  unsigned long long a = (unsigned long long)p;
  u32x4 r = {(unsigned)a, (unsigned)(a >> 32) & 0xffffu, bytes, 0x00020000u};
  return r;
}
DEVINL void dma16(u32x4 rsrc, unsigned voff, unsigned soff, unsigned lds_addr) {
  asm volatile("s_mov_b32 m0, %0\n\ts_nop 0\n\tbuffer_load_dwordx4 %1, %2, %3 offen lds" :: "s"(lds_addr), "v"(voff), "s"(rsrc), "s"(soff) : "memory");
}
DEVINL void dma16x4(u32x4 rsrc, unsigned v0, unsigned v1, unsigned v2, unsigned v3, unsigned soff, unsigned lds_addr) {
  asm volatile("s_mov_b32 m0, %0\n\ts_nop 0\n\t"
               "buffer_load_dwordx4 %1, %5, %6 offen lds\n\t"
               "buffer_load_dwordx4 %2, %5, %6 offen offset:1024 lds\n\t"
               "buffer_load_dwordx4 %3, %5, %6 offen offset:2048 lds\n\t"
               "buffer_load_dwordx4 %4, %5, %6 offen offset:3072 lds"
               :: "s"(lds_addr), "v"(v0), "v"(v1), "v"(v2), "v"(v3), "s"(rsrc), "s"(soff) : "memory");
}
DEVINL void dma16x4_nt(u32x4 rsrc, unsigned v0, unsigned v1, unsigned v2, unsigned v3, unsigned soff, unsigned lds_addr) {
  asm volatile("s_mov_b32 m0, %0\n\ts_nop 0\n\t"
               "buffer_load_dwordx4 %1, %5, %6 offen nt lds\n\t"
               "buffer_load_dwordx4 %2, %5, %6 offen offset:1024 nt lds\n\t"
               "buffer_load_dwordx4 %3, %5, %6 offen offset:2048 nt lds\n\t"
               "buffer_load_dwordx4 %4, %5, %6 offen offset:3072 nt lds"
               :: "s"(lds_addr), "v"(v0), "v"(v1), "v"(v2), "v"(v3), "s"(rsrc), "s"(soff) : "memory");
}
DEVINL void dma16_nt(u32x4 rsrc, unsigned voff, unsigned soff, unsigned lds_addr) {
  asm volatile("s_mov_b32 m0, %0\n\ts_nop 0\n\tbuffer_load_dwordx4 %1, %2, %3 offen nt lds" :: "s"(lds_addr), "v"(voff), "s"(rsrc), "s"(soff) : "memory");
}
DEVINL u32x4 ld16_nt(u32x4 rsrc, unsigned voff, unsigned soff) {
  u32x4 r;
  asm volatile("buffer_load_dwordx4 %0, %1, %2, %3 offen nt" : "=v"(r) : "v"(voff), "s"(rsrc), "s"(soff) : "memory");
  return r;
}
DEVINL u32x4 ld16(u32x4 rsrc, unsigned voff, unsigned soff) {
  u32x4 r;
  asm volatile("buffer_load_dwordx4 %0, %1, %2, %3 offen" : "=v"(r) : "v"(voff), "s"(rsrc), "s"(soff) : "memory");
  return r;
}
DEVINL const lds_char* ldsp(unsigned a) { return (const lds_char*)(size_t)a; }
DEVINL bf16x8 lds_b128(const lds_char* p) { return *LDSP(const bf16x8, p); }
DEVINL bf16x8 lds_tr_frag(const lds_char* p) {
  s16x4 lo = __builtin_amdgcn_ds_read_tr16_b64_v4i16(LDSP(s16x4, p));
  s16x4 hi = __builtin_amdgcn_ds_read_tr16_b64_v4i16(LDSP(s16x4, p + 1024));
  s16x8 r = __builtin_shufflevector(lo, hi, 0, 1, 2, 3, 4, 5, 6, 7);
  return __builtin_bit_cast(bf16x8, r);
}
DEVINL f32x4 mfma(bf16x8 a, bf16x8 b, f32x4 c) { return __builtin_amdgcn_mfma_f32_16x16x32_bf16(a, b, c, 0, 0, 0); }
DEVINL unsigned pk2(float lo, float hi) {
  f32x2 f = {lo, hi};
  return __builtin_bit_cast(unsigned, __builtin_convertvector(f, bf16x2));
}
DEVINL unsigned pk2v(f32x2 f) { return __builtin_bit_cast(unsigned, __builtin_convertvector(f, bf16x2)); }
DEVINL float bf_lo(unsigned u) { return __builtin_bit_cast(float, u << 16); }
DEVINL float bf_hi(unsigned u) { return __builtin_bit_cast(float, u & 0xffff0000u); }
DEVINL unsigned f2u(float f) { return __builtin_bit_cast(unsigned, f); }
DEVINL float u2f(unsigned u) { return __builtin_bit_cast(float, u); }
DEVINL f32x4 unpack4(u32x2 v) { f32x4 r = {bf_lo(v[0]), bf_hi(v[0]), bf_lo(v[1]), bf_hi(v[1])}; return r; }
DEVINL f32x2 lo2(f32x4 v) { return __builtin_shufflevector(v, v, 0, 1); }
DEVINL f32x2 hi2(f32x4 v) { return __builtin_shufflevector(v, v, 2, 3); }
DEVINL f32x2 bc2(float v) { f32x2 r = {v, v}; return r; }
DEVINL f32x2 fma2(f32x2 a, f32x2 b, f32x2 c) { return __builtin_elementwise_fma(a, b, c); }
DEVINL float sfma(float a, float b, float c) { asm volatile("" : "+v"(a)); float d = __builtin_fmaf(a, b, c); asm volatile("" : "+v"(d)); return d; }
DEVINL float smul(float a, float b) { asm volatile("" : "+v"(a)); float d = a * b; asm volatile("" : "+v"(d)); return d; }
DEVINL float sadd(float a, float b) { asm volatile("" : "+v"(a)); float d = a + b; asm volatile("" : "+v"(d)); return d; }
DEVINL void unpack_chunk(u32x4 L, f32x4& t0, f32x4& t1) {
  auto a = __builtin_amdgcn_permlane16_swap(L[0], L[2], false, false);
  auto b = __builtin_amdgcn_permlane16_swap(L[1], L[3], false, false);
  t0[0] = bf_lo(a[0]); t0[1] = bf_hi(a[0]); t0[2] = bf_lo(b[0]); t0[3] = bf_hi(b[0]);
  t1[0] = bf_lo(a[1]); t1[1] = bf_hi(a[1]); t1[2] = bf_lo(b[1]); t1[3] = bf_hi(b[1]);
}
DEVINL u32x4 pack_chunk(f32x4 t0, f32x4 t1) {
  unsigned p00 = pk2(t0[0], t0[1]), p01 = pk2(t0[2], t0[3]);
  unsigned p10 = pk2(t1[0], t1[1]), p11 = pk2(t1[2], t1[3]);
  auto a = __builtin_amdgcn_permlane16_swap(p00, p10, false, false);
  auto b = __builtin_amdgcn_permlane16_swap(p01, p11, false, false);
  u32x4 r = {a[0], b[0], a[1], b[1]};
  return r;
}
DEVINL float reduce4(float a, float b, float c, float d) {
  auto r0 = __builtin_amdgcn_permlane16_swap(f2u(a), f2u(b), false, false);
  auto r1 = __builtin_amdgcn_permlane16_swap(f2u(c), f2u(d), false, false);
  float ab = u2f(r0[0]) + u2f(r0[1]);
  float cd = u2f(r1[0]) + u2f(r1[1]);
  auto r2 = __builtin_amdgcn_permlane32_swap(f2u(ab), f2u(cd), false, false);
  return u2f(r2[0]) + u2f(r2[1]);
}

__global__ __launch_bounds__(NTHREADS, 3)
void controlnet_fused(const __hip_bfloat16* __restrict__ xf, const __hip_bfloat16* __restrict__ x,
                      const __hip_bfloat16* __restrict__ c, const __hip_bfloat16* __restrict__ wz1,
                      const __hip_bfloat16* __restrict__ wz2, const __hip_bfloat16* __restrict__ wconv,
                      __hip_bfloat16* __restrict__ out
                      ) {
  extern __shared__ __attribute__((aligned(16))) char smem_generic[];
  const unsigned lds0 = (unsigned)(size_t)(lds_char*)smem_generic;

  const int tid = threadIdx.x;
  const int lane = tid & 63;
  const int wv = __builtin_amdgcn_readfirstlane(tid >> 6);
  const int q = lane >> 4, px = lane & 15;
  const int bid = blockIdx.x;
  const int tile = (bid & 7) * 32 + (bid >> 3);
  const int bimg = tile >> 7, ty0 = ((tile >> 3) & 15) * 8, tx0 = (tile & 7) * 16;
  const u32x4 r_c  = make_rsrc(c, TENSOR_BYTES), r_x = make_rsrc(x, TENSOR_BYTES);

  auto grp_row = [&](int T, int j) LAMBDA -> int { return T == 0 ? j : (j == 0 ? 9 : j + 4); };
  auto row_soff = [&](int r) LAMBDA -> unsigned {
    const int gy = ty0 - 1 + r;
    return (unsigned)gy < 128u ? (unsigned)(((bimg * 128 + gy) * 128 + tx0) * 256) : OOB;
  };
  auto row_voff = [&](int w_) LAMBDA -> unsigned { const int p = 4 * w_ + q; return (unsigned)(p * 256 + ((px ^ p) * 16)); };
  auto col_voff = [&](int T, int w_) LAMBDA -> unsigned {
    const int p = 4 * w_ + q;
    const int gy = ty0 - 1 + 5 * T + (p >> 1), gx = tx0 - 1 + ((p & 1) ? 17 : 0);
    const bool ok = p < 10 && (unsigned)gy < 128u && (unsigned)gx < 128u;
    return ok ? (unsigned)(((bimg * 128 + gy) * 128 + gx) * 256 + ((px ^ p) * 16)) : OOB;
  };

  if (wv == 8) {
    unsigned rv[4];
#pragma unroll
    for (int w_ = 0; w_ < 4; ++w_) rv[w_] = row_voff(w_) - w_ * 1024;
    auto rows = [&](int j) LAMBDA {
#pragma unroll
      for (int T = 0; T < 2; ++T) {
        const unsigned so = row_soff(grp_row(T, j));
        const unsigned dst = lds0 + reg_off(j >> 1) + (T * 2 + (j & 1)) * 8192;
        dma16x4_nt(r_c, rv[0], rv[1], rv[2], rv[3], so, dst);
        dma16x4_nt(r_x, rv[0], rv[1], rv[2], rv[3], so, dst + 4096);
      }
    };
    unsigned tw[4];
    {
      const u32x4 r_xf0 = make_rsrc(xf, TENSOR_BYTES);
      const unsigned to = (unsigned)(((bimg * 128 + ty0 + (lane >> 5)) * 128 + tx0 + ((lane >> 1) & 15)) * 256 + (lane & 1) * 128);
#pragma unroll
      for (int i = 0; i < 4; ++i)
        asm volatile("buffer_load_dword %0, %1, %2, %3 offen" : "=v"(tw[i]) : "v"(to), "s"(r_xf0), "s"(i * 65536) : "memory");
    }
    BAR();
    rows(2); rows(3);
    BAR();
    rows(4);
#pragma unroll
    for (int T = 0; T < 2; ++T) {
      unsigned cv[4];
#pragma unroll
      for (int w_ = 0; w_ < 4; ++w_) { unsigned o = col_voff(T, w_); cv[w_] = o == OOB ? OOB : o - w_ * 1024; }
      const unsigned dst = lds0 + R2 + (T * 2 + 1) * 8192;
      dma16x4_nt(r_c, cv[0], cv[1], cv[2], cv[3], 0, dst);
      dma16x4_nt(r_x, cv[0], cv[1], cv[2], cv[3], 0, dst + 4096);
    }
    const u32x4 r_w2 = make_rsrc(wz2, 32768), r_wc = make_rsrc(wconv, 9 * 32768);
    const unsigned vA = (unsigned)(q * 256 + ((px ^ (q << 1)) * 16));
    const unsigned vB = (unsigned)(q * 256 + ((px ^ ((q | 4) << 1)) * 16));
    auto block = [&](u32x4 rs, int src, int dst) LAMBDA {
      dma16x4(rs, vA, vA, vB, vB, src, lds0 + dst);
      dma16x4(rs, vA, vA, vB, vB, src + 4096, lds0 + dst + 4096);
    };
    auto stage = [&](int s) LAMBDA {
#pragma unroll
      for (int di = 0; di < 3; ++di) block(r_wc, (di * 3 + step_dj(s)) * 32768 + (s & 3) * 8192, blk_off(s, di));
    };
    WAIT_VM(32); BAR();
    stage(0);
    WAIT_VM(24); BAR();
    stage(1);
    WAIT_VM(24); BAR();
    stage(2); stage(3);
    WAIT_VM(48); BAR();
    stage(4);
    WAIT_VM(24); BAR();
    stage(5);
    WAIT_VM(0); BAR();
    stage(6); stage(7);
    WAIT_VM(0); BAR();
    stage(8); stage(9);
    WAIT_VM(0); BAR();
    stage(10); stage(11);
    WAIT_VM(0); BAR();
    block(r_w2, 0, wz2_off(0)); block(r_w2, 8192, wz2_off(1)); block(r_w2, 16384, wz2_off(2)); block(r_w2, 24576, wz2_off(3));
    WAIT_VM(0); BAR();
    asm volatile("" :: "v"(tw[0]), "v"(tw[1]), "v"(tw[2]), "v"(tw[3]));
    return;
  }

  const int T = wv >> 2, w = wv & 3;
  STAMP(0);
  {
    const u32x4 r_w1 = make_rsrc(wz1, 32768);
    const int wk = 4 * wv + q;
    const int wf = (wk & 3) | (((wk >> 3) & 1) << 2);
    const unsigned wvoff = (unsigned)(wk * 256 + ((px ^ (wf << 1)) * 16));
#pragma unroll
    for (int ks = 0; ks < 4; ++ks) dma16(r_w1, wvoff, ks * 8192, lds0 + wv * 1024 + H_OFF + ks * 8192);
    const unsigned rv = row_voff(w);
#pragma unroll
    for (int j = 0; j < 2; ++j) {
      const unsigned so = row_soff(grp_row(T, j));
      const unsigned dst = lds0 + R0 + (T * 2 + j) * 8192 + w * 1024;
      dma16_nt(r_c, rv, so, dst);
      dma16_nt(r_x, rv, so, dst + 4096);
    }
  }
  STAMP(13);
  const u32x4 r_xf = make_rsrc(xf, TENSOR_BYTES);
  __amdgpu_buffer_rsrc_t r_o  = __builtin_amdgcn_make_buffer_rsrc((void*)out, 0, TENSOR_BYTES, 0x00020000);
  const int aq = (lane & 15) >> 2, ap = lane & 3;
  const int af = aq | ((q & 1) << 2);
  const unsigned ab0 = (unsigned)((8 * q + aq) * 256 + (((2 * w) ^ af) * 32) + 8 * ap);
  const unsigned aH0 = lds0 + ab0, aH1 = lds0 + (ab0 ^ 32);
  const unsigned aR0 = aH0 + R0, aR1 = aH1 + R0;
  const unsigned aS0 = aH0 + R2, aS1 = aH1 + R2;
  auto aptr = [&](int off, int t) LAMBDA -> const lds_char* {
    if (off < R0) return ldsp(t ? aH1 : aH0) + off;
    if (off < R2) return ldsp(t ? aR1 : aR0) + (off - R0);
    return ldsp(t ? aS1 : aS0) + (off - R2);
  };
  unsigned hw[6];
  {
    const unsigned hwbase = lds0 + H_OFF + (4 * w + (q >> 1)) * PLANE + (q & 1) * 8;
#pragma unroll
    for (int j = 0; j < 5; ++j) hw[j] = hwbase + (grp_row(T, j) * 18 + px + 1) * 16;
    hw[5] = hwbase + (px < 10 ? (5 * T + (px >> 1)) * 18 + ((px & 1) ? 17 : 0) : 180 + 6 * T + (px - 10)) * 16;
  }
  unsigned cb[4], cb2[4];
#pragma unroll
  for (int ks = 0; ks < 4; ++ks) { cb[ks] = lds0 + R0 + T * 16384 + px * 256 + (((4 * ks + q) ^ px) * 16); cb2[ks] = cb[ks] + 65536; }
  const unsigned xb0 = lds0 + R0 + T * 16384 + 4096 + px * 256 + (((4 * w + (q >> 1)) ^ px) * 16) + (q & 1) * 8;
  const unsigned xb1 = lds0 + R0 + T * 16384 + 4096 + px * 256 + (((4 * w + 2 + (q >> 1)) ^ px) * 16) + (q & 1) * 8;
  const unsigned stw = lds0 + STATS_OFF + (w >> 1) * 3072 + ((T * 6 + (q >> 1)) * 16 + px) * 16 + (w & 1) * 8 + (q & 1) * 4;
  const unsigned str = lds0 + STATS_OFF + (T * 6 * 16 + px) * 16;

  f32x4 hin[6][2];
  bf16x8 A1f[4][2];
  bf16x8 Af[2][3][2], Bf[2][6];
  u32x4 xfr[4];
  bf16x8 Cb[2][4]; u32x2 Xb[2][2];
  auto z1_read = [&](auto kc) LAMBDA {
    constexpr int k = decltype(kc)::value;
#pragma unroll
    for (int jj = 0; jj < 2; ++jj) {
      if constexpr (k < 2) {
        Xb[jj][0] = *LDSP(const u32x2, ldsp(xb0) + k * 32768 + jj * 8192);
        Xb[jj][1] = *LDSP(const u32x2, ldsp(xb1) + k * 32768 + jj * 8192);
      } else {
        Xb[jj][0] = *LDSP(const u32x2, ldsp(xb0 + 65536) + jj * 8192);
        Xb[jj][1] = *LDSP(const u32x2, ldsp(xb1 + 65536) + jj * 8192);
      }
    }
#pragma unroll
    for (int ks = 0; ks < 4; ++ks)
#pragma unroll
      for (int jj = 0; jj < 2; ++jj)
        Cb[jj][ks] = k < 2 ? lds_b128(ldsp(cb[ks]) + k * 32768 + jj * 8192) : lds_b128(ldsp(cb2[ks]) + jj * 8192);
  };
  auto z1_unpack = [&](auto kc) LAMBDA {
    constexpr int k = decltype(kc)::value;
#pragma unroll
    for (int jj = 0; jj < 2; ++jj) {
      hin[2 * k + jj][0] = unpack4(Xb[jj][0]);
      hin[2 * k + jj][1] = unpack4(Xb[jj][1]);
    }
  };
  auto z1_mma1 = [&](auto kc, auto ic) LAMBDA {
    constexpr int k = decltype(kc)::value, i = decltype(ic)::value;
    constexpr int ks = i >> 2, jj = (i >> 1) & 1, t = i & 1;
    hin[2 * k + jj][t] = mfma(A1f[ks][t], Cb[jj][ks], hin[2 * k + jj][t]);
  };
  auto z1_stats = [&](auto kc) LAMBDA {
    constexpr int k = decltype(kc)::value;
    float st[4];
#pragma unroll
    for (int jj = 0; jj < 2; ++jj) {
      const int j = 2 * k + jj;
      f32x2 a = (lo2(hin[j][0]) + hi2(hin[j][0])) + (lo2(hin[j][1]) + hi2(hin[j][1]));
      f32x2 b = lo2(hin[j][0]) * lo2(hin[j][0]);
      b = fma2(hi2(hin[j][0]), hi2(hin[j][0]), b);
      b = fma2(lo2(hin[j][1]), lo2(hin[j][1]), b);
      b = fma2(hi2(hin[j][1]), hi2(hin[j][1]), b);
      st[2 * jj] = a[0] + a[1];
      st[2 * jj + 1] = b[0] + b[1];
    }
    float r = reduce4(st[0], st[1], st[2], st[3]);
    *LDSP(float, (lds_char*)(size_t)stw + 2 * k * 256) = r;
  };
  f32x4 sp0, sp1;
  const unsigned strh = str + (q >> 1) * 256;
  auto si_read = [&](auto kc) LAMBDA {
    constexpr int k = decltype(kc)::value;
    sp0 = *LDSP(const f32x4, ldsp(strh) + 2 * k * 256);
    sp1 = *LDSP(const f32x4, ldsp(strh) + 2 * k * 256 + 3072);
  };
  auto si_math = [&](auto kc, auto ngc, auto hook) LAMBDA {
    constexpr int k = decltype(kc)::value, NG = decltype(ngc)::value, NQ = 2 * NG;
    float rs2[2], nb2[2];
    {
      f32x2 m = ((lo2(sp0) + hi2(sp0)) + (lo2(sp1) + hi2(sp1))) * bc2(1.f / 128.f);
      float var = __builtin_fmaxf(__builtin_fmaf(-m[0], m[0], m[1]), 0.f);
      float r = __builtin_amdgcn_rsqf(var + 1e-5f) * -1.4426950408889634f;
      float n = -m[0] * r;
      auto a = __builtin_amdgcn_permlane32_swap(f2u(r), f2u(r), false, false);
      auto b = __builtin_amdgcn_permlane32_swap(f2u(n), f2u(n), false, false);
      rs2[0] = u2f(a[0]); rs2[1] = u2f(a[1]); nb2[0] = u2f(b[0]); nb2[1] = u2f(b[1]);
    }
    f32x2 t[2 * NQ], e[2 * NQ];
#pragma unroll
    for (int g = 0; g < NQ; ++g) {
      t[2 * g]     = fma2(lo2(hin[2 * k + (g >> 1)][g & 1]), bc2(rs2[g >> 1]), bc2(nb2[g >> 1]));
      t[2 * g + 1] = fma2(hi2(hin[2 * k + (g >> 1)][g & 1]), bc2(rs2[g >> 1]), bc2(nb2[g >> 1]));
    }
    SCHED();
    static_for<0, 4 * NQ>([&](auto ic) LAMBDA {
      constexpr int i = decltype(ic)::value;
      hook(ic);
      e[i >> 1][i & 1] = __builtin_amdgcn_exp2f(t[i >> 1][i & 1]);
      SCHED();
    });
    f32x2 P[NQ]; float rr[NQ];
#pragma unroll
    for (int g = 0; g < NQ; ++g) { f32x2 g1 = e[2 * g] + bc2(1.f); P[g] = fma2(g1, e[2 * g + 1], g1); }
#pragma unroll
    for (int g = 0; g < NQ; ++g) rr[g] = __builtin_amdgcn_rcpf(P[g][0] * P[g][1]);
#pragma unroll
    for (int g = 0; g < NQ; ++g) { t[2 * g] = fma2(t[2 * g], e[2 * g + 1], t[2 * g]); t[2 * g + 1] = fma2(t[2 * g + 1], e[2 * g], t[2 * g + 1]); }
#pragma unroll
    for (int g = 0; g < NQ; ++g) {
      f32x2 sw = __builtin_shufflevector(P[g], P[g], 1, 0);
      P[g] = sw * bc2(rr[g] * -0.6931471805599453f);
    }
#pragma unroll
    for (int g = 0; g < NQ; ++g) {
      u32x2 pk = {pk2v(t[2 * g] * P[g]), pk2v(t[2 * g + 1] * P[g])};
      *LDSP(u32x2, (lds_char*)(size_t)hw[2 * k + (g >> 1)] + 2 * (g & 1) * PLANE) = pk;
    }
  };
  WAIT_VM(4);
  BAR();
#pragma unroll
  for (int ks = 0; ks < 4; ++ks) {
    A1f[ks][0] = lds_tr_frag(aptr(H_OFF + ks * 8192, 0));
    A1f[ks][1] = lds_tr_frag(aptr(H_OFF + ks * 8192, 1));
  }
  WAIT_VM(0);
  BAR();
  STAMP(1);
  z1_read(IC<0>{});
  SCHED();
  z1_unpack(IC<0>{});
  static_for<0, 16>([&](auto ic) LAMBDA { z1_mma1(IC<0>{}, ic); });
  z1_stats(IC<0>{});
  SCHED();
  STAMP(2);
  BAR();
  STAMP(3);
  si_read(IC<0>{});
  z1_read(IC<1>{});
  SCHED();
  z1_unpack(IC<1>{});
  si_math(IC<0>{}, IC<2>{}, [&](auto ic) LAMBDA { z1_mma1(IC<1>{}, ic); });
  z1_stats(IC<1>{});
  SCHED();
  STAMP(4);
  BAR();
  STAMP(5);
  si_read(IC<1>{});
  z1_read(IC<2>{});
  const unsigned xoff = (unsigned)(px * 256 + (((q & 1) << 1) | (q >> 1)) * 16);
  const unsigned xsoff = (unsigned)(((bimg * 128 + ty0 + wv) * 128 + tx0) * 256);
  SCHED();
  z1_unpack(IC<2>{});
  si_math(IC<1>{}, IC<2>{}, [&](auto ic) LAMBDA { z1_mma1(IC<2>{}, ic); });
  z1_stats(IC<2>{});
  SCHED();
  STAMP(6);
  BAR();
  STAMP(7);
  si_read(IC<2>{});
#pragma unroll
  for (int di = 0; di < 3; ++di) {
    Af[0][di][0] = lds_tr_frag(aptr(blk_off(0, di), 0));
    Af[0][di][1] = lds_tr_frag(aptr(blk_off(0, di), 1));
  }
  SCHED();
  si_math(IC<2>{}, IC<2>{}, [&](auto ic) LAMBDA {});
  f32x4 acc[4][2];
#pragma unroll
  for (int i = 0; i < 4; ++i) { acc[i][0] = hin[i + 1][0]; acc[i][1] = hin[i + 1][1]; }
  SCHED();
  STAMP(8);
  BAR();
  STAMP(9);

  const lds_char* hb = ldsp(lds0 + H_OFF + q * PLANE + (T * 4 * 18 + px) * 16);
#pragma unroll
  for (int rr = 0; rr < 6; ++rr) Bf[0][rr] = lds_b128(hb + (rr * 18 + step_dj(0)) * 16);
  s16x4 Ah[2][3][2][2];
#pragma unroll
  for (int di = 0; di < 3; ++di)
#pragma unroll
    for (int t = 0; t < 2; ++t) {
      s16x8 v = __builtin_bit_cast(s16x8, Af[0][di][t]);
      Ah[0][di][t][0] = __builtin_shufflevector(v, v, 0, 1, 2, 3);
      Ah[0][di][t][1] = __builtin_shufflevector(v, v, 4, 5, 6, 7);
    }
  auto afrag = [&](int buf, int di, int t) LAMBDA -> bf16x8 {
    return __builtin_bit_cast(bf16x8, __builtin_shufflevector(Ah[buf][di][t][0], Ah[buf][di][t][1], 0, 1, 2, 3, 4, 5, 6, 7));
  };
  bf16x8 A2f[8];
  f32x4 o2[8];
  const unsigned abz = lds0 + (unsigned)((8 * q + aq) * 256 + (af * 32) + 8 * ap);
  const unsigned owbase = lds0 + (w < 3 ? R2 + w * 8192 : R1 + 8192) + (q >> 1) * HOUT_PLANE + (q & 1) * 8 + (4 * T * 16 + px) * 16;
  static_for<0, 12>([&](auto sc) LAMBDA {
    constexpr int s = decltype(sc)::value;
    constexpr int cur = s & 1, nxt = cur ^ 1;
    SCHED();
    if constexpr ((s & 1) == 1) BAR();
    if constexpr (s == 10) {
      asm volatile("s_waitcnt vmcnt(0)" : "+v"(xfr[0]), "+v"(xfr[1]), "+v"(xfr[2]), "+v"(xfr[3]) :: "memory");
    }
    SCHED();
    static_for<0, 24>([&](auto mc) LAMBDA {
      constexpr int m = decltype(mc)::value;
      constexpr int di = s < 11 ? m / 8 : (m % 6) / 2, i = s < 11 ? (m % 8) / 2 : m / 6, t = m & 1;
      acc[i][t] = mfma(afrag(cur, di, t), Bf[cur][i + di], acc[i][t]);
      if constexpr (s + 1 < 12) {
        constexpr int s1 = s + 1, dj = step_dj(s1), ks = s1 & 3;
        if constexpr (m < 12) {
          constexpr int rdi = m >> 2, rt = (m >> 1) & 1, rh = m & 1;
          Ah[nxt][rdi][rt][rh] = __builtin_amdgcn_ds_read_tr16_b64_v4i16(LDSP(s16x4, aptr(blk_off(s1, rdi), rt) + rh * 1024));
        } else if constexpr (m < 18) {
          constexpr int rr = m - 12;
          Bf[nxt][rr] = lds_b128(hb + ks * 4 * PLANE + (rr * 18 + dj) * 16);
        }
      } else {
        if constexpr (m < 8) A2f[m] = lds_tr_frag(ldsp((abz ^ (m * 32)) + R0));
      }
      if constexpr ((s == 2 || s == 4 || s == 6 || s == 8) && m == 4) xfr[s / 2 - 1] = ld16(r_xf, xoff, xsoff + (s / 2 - 1) * 64);
      if constexpr (s == 10 && (m % 6) == 1) unpack_chunk(xfr[m / 6], o2[2 * (m / 6)], o2[2 * (m / 6) + 1]);
      if constexpr (s == 11 && m >= 8 && (m % 6) == 2) {
        constexpr int r = m / 6 - 1;
#pragma unroll
        for (int tt = 0; tt < 2; ++tt) {
          u32x2 pk = {pk2(acc[r][tt][0], acc[r][tt][1]), pk2(acc[r][tt][2], acc[r][tt][3])};
          *LDSP(u32x2, (lds_char*)(size_t)owbase + 2 * tt * HOUT_PLANE + r * 256) = pk;
        }
      }
      SCHED();
    });
  });
#pragma unroll
  for (int tt = 0; tt < 2; ++tt) {
    u32x2 pk = {pk2(acc[3][tt][0], acc[3][tt][1]), pk2(acc[3][tt][2], acc[3][tt][3])};
    *LDSP(u32x2, (lds_char*)(size_t)owbase + 2 * tt * HOUT_PLANE + 3 * 256) = pk;
  }
  SCHED();
  STAMP(10);
  BAR();
  STAMP(11);
  {
    bf16x8 B[4];
    const unsigned hr = lds0 + q * HOUT_PLANE + (wv * 16 + px) * 16;
#pragma unroll
    for (int ks = 0; ks < 4; ++ks) B[ks] = lds_b128(ldsp(hr + hout_off(ks)));
    bf16x8 A2n[8];
    static_for<0, 4>([&](auto kc) LAMBDA {
      constexpr int ks = decltype(kc)::value;
      static_for<0, 8>([&](auto tc) LAMBDA {
        constexpr int tt = decltype(tc)::value;
        if constexpr (ks + 1 < 4) A2n[tt] = lds_tr_frag(ldsp((abz ^ (tt * 32)) + wz2_off(ks + 1)));
        o2[tt] = mfma(A2f[tt], B[ks], o2[tt]);
        SCHED();
      });
#pragma unroll
      for (int tt = 0; tt < 8; ++tt) A2f[tt] = A2n[tt];
    });
    const unsigned ow = lds0 + H_OFF + wv * 4096 + px * 256 + (q & 1) * 8;
#pragma unroll
    for (int tt = 0; tt < 8; ++tt) {
      u32x2 p = {pk2(o2[tt][0], o2[tt][1]), pk2(o2[tt][2], o2[tt][3])};
      *LDSP(u32x2, (lds_char*)(size_t)(ow + (((2 * tt + (q >> 1)) ^ px) * 16))) = p;
    }
    const unsigned orr = lds0 + H_OFF + wv * 4096 + q * 256;
    u32x4 ov[4];
#pragma unroll
    for (int g = 0; g < 4; ++g) ov[g] = *LDSP(const u32x4, ldsp(orr + ((px ^ (4 * g + q)) * 16)) + g * 1024);
#pragma unroll
    for (int g = 0; g < 4; ++g)
      __builtin_amdgcn_raw_buffer_store_b128(ov[g], r_o, (unsigned)(q * 256 + px * 16), xsoff + g * 1024, STORE_AUX);
  }
  STAMP(12);
}

extern "C" void kernel_launch(void* const* d_in, const int* in_sizes, int n_in,
                              void* d_out, int out_size, void* d_ws, size_t ws_size,
                              hipStream_t stream) {
  const __hip_bfloat16* xf  = (const __hip_bfloat16*)d_in[0];
  const __hip_bfloat16* x   = (const __hip_bfloat16*)d_in[1];
  const __hip_bfloat16* c   = (const __hip_bfloat16*)d_in[2];
  const __hip_bfloat16* wz1 = (const __hip_bfloat16*)d_in[3];
  const __hip_bfloat16* wz2 = (const __hip_bfloat16*)d_in[4];
  const __hip_bfloat16* wc  = (const __hip_bfloat16*)d_in[5];
  (void)hipFuncSetAttribute((const void*)controlnet_fused, hipFuncAttributeMaxDynamicSharedMemorySize, LDS_BYTES);
  controlnet_fused<<<256, NTHREADS, LDS_BYTES, stream>>>(xf, x, c, wz1, wz2, wc, (__hip_bfloat16*)d_out
  );
}
```

```cpp
#include <hip/hip_runtime.h>
#include <hip/hip_bf16.h>

typedef __attribute__((ext_vector_type(8))) __bf16 bf16x8;
typedef __attribute__((ext_vector_type(2))) __bf16 bf16x2;
typedef __attribute__((ext_vector_type(4))) short s16x4;
typedef __attribute__((ext_vector_type(8))) short s16x8;
typedef __attribute__((ext_vector_type(4))) float f32x4;
typedef __attribute__((ext_vector_type(2))) float f32x2;
typedef __attribute__((ext_vector_type(4))) unsigned u32x4;
typedef __attribute__((ext_vector_type(2))) unsigned u32x2;
typedef __attribute__((address_space(3))) char lds_char;
#define LDSP(T, p) ((__attribute__((address_space(3))) T*)(p))
#define DEVINL __device__ __forceinline__
#define LAMBDA __attribute__((always_inline))

constexpr int PLANE     = 3072;
constexpr int H_OFF     = 0;
constexpr int R0 = 49152, R1 = 81920, R2 = 114688;
constexpr int STATS_OFF = 147456;
constexpr int XF_OFF    = STATS_OFF;
constexpr int LDS_BYTES = STATS_OFF + 16384;
constexpr int HOUT_PLANE = 2048;
__host__ __device__ constexpr int reg_off(int a) { return a == 0 ? R0 : a == 1 ? R1 : R2; }
__host__ __device__ constexpr int blk_off(int s, int di) { return (s & 3) < 3 ? reg_off(s & 3) + di * 8192 : reg_off(di) + 24576; }
__host__ __device__ constexpr int step_dj(int s) { return s >> 2; }
__host__ __device__ constexpr int wz2_off(int ks) { return ks < 3 ? R0 + ks * 8192 : R1; }
__host__ __device__ constexpr int hout_off(int ks) { return ks < 3 ? R2 + ks * 8192 : R1 + 8192; }
constexpr unsigned OOB  = 0x40000000u;
constexpr int TENSOR_BYTES = 2 * 128 * 128 * 128 * 2;
constexpr int NTHREADS = 576;

#define STAMP(i) do {} while (0)
#define TAIL_MODE 2
#define STORE_AUX 17
#define WAIT_VM(n) asm volatile("s_waitcnt vmcnt(" #n ")" ::: "memory")
#define BAR() asm volatile("s_waitcnt lgkmcnt(0)\n\ts_barrier" ::: "memory")
#define SCHED() __builtin_amdgcn_sched_barrier(0)

template <int N> struct IC { static constexpr int value = N; };
template <int I, int N, class F> DEVINL void static_for(F&& f) { if constexpr (I < N) { f(IC<I>{}); static_for<I + 1, N>(f); } }

DEVINL u32x4 make_rsrc(const void* p, unsigned bytes) {
  unsigned long long a = (unsigned long long)p;
  u32x4 r = {(unsigned)a, (unsigned)(a >> 32) & 0xffffu, bytes, 0x00020000u};
  return r;
}
DEVINL void dma16(u32x4 rsrc, unsigned voff, unsigned soff, unsigned lds_addr) {
  asm volatile("s_mov_b32 m0, %0\n\ts_nop 0\n\tbuffer_load_dwordx4 %1, %2, %3 offen lds" :: "s"(lds_addr), "v"(voff), "s"(rsrc), "s"(soff) : "memory");
}
DEVINL void dma16x4(u32x4 rsrc, unsigned v0, unsigned v1, unsigned v2, unsigned v3, unsigned soff, unsigned lds_addr) {
  asm volatile("s_mov_b32 m0, %0\n\ts_nop 0\n\t"
               "buffer_load_dwordx4 %1, %5, %6 offen lds\n\t"
               "buffer_load_dwordx4 %2, %5, %6 offen offset:1024 lds\n\t"
               "buffer_load_dwordx4 %3, %5, %6 offen offset:2048 lds\n\t"
               "buffer_load_dwordx4 %4, %5, %6 offen offset:3072 lds"
               :: "s"(lds_addr), "v"(v0), "v"(v1), "v"(v2), "v"(v3), "s"(rsrc), "s"(soff) : "memory");
}
DEVINL void dma16x4_nt(u32x4 rsrc, unsigned v0, unsigned v1, unsigned v2, unsigned v3, unsigned soff, unsigned lds_addr) {
  asm volatile("s_mov_b32 m0, %0\n\ts_nop 0\n\t"
               "buffer_load_dwordx4 %1, %5, %6 offen nt lds\n\t"
               "buffer_load_dwordx4 %2, %5, %6 offen offset:1024 nt lds\n\t"
               "buffer_load_dwordx4 %3, %5, %6 offen offset:2048 nt lds\n\t"
               "buffer_load_dwordx4 %4, %5, %6 offen offset:3072 nt lds"
               :: "s"(lds_addr), "v"(v0), "v"(v1), "v"(v2), "v"(v3), "s"(rsrc), "s"(soff) : "memory");
}
DEVINL void dma16_nt(u32x4 rsrc, unsigned voff, unsigned soff, unsigned lds_addr) {
  asm volatile("s_mov_b32 m0, %0\n\ts_nop 0\n\tbuffer_load_dwordx4 %1, %2, %3 offen nt lds" :: "s"(lds_addr), "v"(voff), "s"(rsrc), "s"(soff) : "memory");
}
DEVINL u32x4 ld16_nt(u32x4 rsrc, unsigned voff, unsigned soff) {
  u32x4 r;
  asm volatile("buffer_load_dwordx4 %0, %1, %2, %3 offen nt" : "=v"(r) : "v"(voff), "s"(rsrc), "s"(soff) : "memory");
  return r;
}
DEVINL u32x4 ld16(u32x4 rsrc, unsigned voff, unsigned soff) {
  u32x4 r;
  asm volatile("buffer_load_dwordx4 %0, %1, %2, %3 offen" : "=v"(r) : "v"(voff), "s"(rsrc), "s"(soff) : "memory");
  return r;
}
DEVINL const lds_char* ldsp(unsigned a) { return (const lds_char*)(size_t)a; }
DEVINL bf16x8 lds_b128(const lds_char* p) { return *LDSP(const bf16x8, p); }
DEVINL bf16x8 lds_tr_frag(const lds_char* p) {
  s16x4 lo = __builtin_amdgcn_ds_read_tr16_b64_v4i16(LDSP(s16x4, p));
  s16x4 hi = __builtin_amdgcn_ds_read_tr16_b64_v4i16(LDSP(s16x4, p + 1024));
  s16x8 r = __builtin_shufflevector(lo, hi, 0, 1, 2, 3, 4, 5, 6, 7);
  return __builtin_bit_cast(bf16x8, r);
}
DEVINL f32x4 mfma(bf16x8 a, bf16x8 b, f32x4 c) { return __builtin_amdgcn_mfma_f32_16x16x32_bf16(a, b, c, 0, 0, 0); }
DEVINL unsigned pk2(float lo, float hi) {
  f32x2 f = {lo, hi};
  return __builtin_bit_cast(unsigned, __builtin_convertvector(f, bf16x2));
}
DEVINL unsigned pk2v(f32x2 f) { return __builtin_bit_cast(unsigned, __builtin_convertvector(f, bf16x2)); }
DEVINL float bf_lo(unsigned u) { return __builtin_bit_cast(float, u << 16); }
DEVINL float bf_hi(unsigned u) { return __builtin_bit_cast(float, u & 0xffff0000u); }
DEVINL unsigned f2u(float f) { return __builtin_bit_cast(unsigned, f); }
DEVINL float u2f(unsigned u) { return __builtin_bit_cast(float, u); }
DEVINL f32x4 unpack4(u32x2 v) { f32x4 r = {bf_lo(v[0]), bf_hi(v[0]), bf_lo(v[1]), bf_hi(v[1])}; return r; }
DEVINL f32x2 lo2(f32x4 v) { return __builtin_shufflevector(v, v, 0, 1); }
DEVINL f32x2 hi2(f32x4 v) { return __builtin_shufflevector(v, v, 2, 3); }
DEVINL f32x2 bc2(float v) { f32x2 r = {v, v}; return r; }
DEVINL f32x2 fma2(f32x2 a, f32x2 b, f32x2 c) { return __builtin_elementwise_fma(a, b, c); }
DEVINL float sfma(float a, float b, float c) { asm volatile("" : "+v"(a)); float d = __builtin_fmaf(a, b, c); asm volatile("" : "+v"(d)); return d; }
DEVINL float smul(float a, float b) { asm volatile("" : "+v"(a)); float d = a * b; asm volatile("" : "+v"(d)); return d; }
DEVINL float sadd(float a, float b) { asm volatile("" : "+v"(a)); float d = a + b; asm volatile("" : "+v"(d)); return d; }
DEVINL void unpack_chunk(u32x4 L, f32x4& t0, f32x4& t1) {
  auto a = __builtin_amdgcn_permlane16_swap(L[0], L[2], false, false);
  auto b = __builtin_amdgcn_permlane16_swap(L[1], L[3], false, false);
  t0[0] = bf_lo(a[0]); t0[1] = bf_hi(a[0]); t0[2] = bf_lo(b[0]); t0[3] = bf_hi(b[0]);
  t1[0] = bf_lo(a[1]); t1[1] = bf_hi(a[1]); t1[2] = bf_lo(b[1]); t1[3] = bf_hi(b[1]);
}
DEVINL u32x4 pack_chunk(f32x4 t0, f32x4 t1) {
  unsigned p00 = pk2(t0[0], t0[1]), p01 = pk2(t0[2], t0[3]);
  unsigned p10 = pk2(t1[0], t1[1]), p11 = pk2(t1[2], t1[3]);
  auto a = __builtin_amdgcn_permlane16_swap(p00, p10, false, false);
  auto b = __builtin_amdgcn_permlane16_swap(p01, p11, false, false);
  u32x4 r = {a[0], b[0], a[1], b[1]};
  return r;
}
DEVINL float reduce4(float a, float b, float c, float d) {
  auto r0 = __builtin_amdgcn_permlane16_swap(f2u(a), f2u(b), false, false);
  auto r1 = __builtin_amdgcn_permlane16_swap(f2u(c), f2u(d), false, false);
  float ab = u2f(r0[0]) + u2f(r0[1]);
  float cd = u2f(r1[0]) + u2f(r1[1]);
  auto r2 = __builtin_amdgcn_permlane32_swap(f2u(ab), f2u(cd), false, false);
  return u2f(r2[0]) + u2f(r2[1]);
}

__global__ __launch_bounds__(NTHREADS, 3)
void controlnet_fused(const __hip_bfloat16* __restrict__ xf, const __hip_bfloat16* __restrict__ x,
                      const __hip_bfloat16* __restrict__ c, const __hip_bfloat16* __restrict__ wz1,
                      const __hip_bfloat16* __restrict__ wz2, const __hip_bfloat16* __restrict__ wconv,
                      __hip_bfloat16* __restrict__ out
                      ) {
  extern __shared__ __attribute__((aligned(16))) char smem_generic[];
  const unsigned lds0 = (unsigned)(size_t)(lds_char*)smem_generic;

  const int tid = threadIdx.x;
  const int lane = tid & 63;
  const int wv = __builtin_amdgcn_readfirstlane(tid >> 6);
  const int q = lane >> 4, px = lane & 15;
  const int bid = blockIdx.x;
  const int tile = (bid & 7) * 32 + (bid >> 3);
  const int bimg = tile >> 7, ty0 = ((tile >> 3) & 15) * 8, tx0 = (tile & 7) * 16;
  const u32x4 r_c  = make_rsrc(c, TENSOR_BYTES), r_x = make_rsrc(x, TENSOR_BYTES);

  auto grp_row = [&](int T, int j) LAMBDA -> int { return T == 0 ? j : (j == 0 ? 9 : j + 4); };
  auto row_soff = [&](int r) LAMBDA -> unsigned {
    const int gy = ty0 - 1 + r;
    return (unsigned)gy < 128u ? (unsigned)(((bimg * 128 + gy) * 128 + tx0) * 256) : OOB;
  };
  auto row_voff = [&](int w_) LAMBDA -> unsigned { const int p = 4 * w_ + q; return (unsigned)(p * 256 + ((px ^ p) * 16)); };
  auto col_voff = [&](int T, int w_) LAMBDA -> unsigned {
    const int p = 4 * w_ + q;
    const int gy = ty0 - 1 + 5 * T + (p >> 1), gx = tx0 - 1 + ((p & 1) ? 17 : 0);
    const bool ok = p < 10 && (unsigned)gy < 128u && (unsigned)gx < 128u;
    return ok ? (unsigned)(((bimg * 128 + gy) * 128 + gx) * 256 + ((px ^ p) * 16)) : OOB;
  };

  if (wv == 8) {
    unsigned rv[4];
#pragma unroll
    for (int w_ = 0; w_ < 4; ++w_) rv[w_] = row_voff(w_) - w_ * 1024;
    auto rows = [&](int j) LAMBDA {
#pragma unroll
      for (int T = 0; T < 2; ++T) {
        const unsigned so = row_soff(grp_row(T, j));
        const unsigned dst = lds0 + reg_off(j >> 1) + (T * 2 + (j & 1)) * 8192;
        dma16x4_nt(r_c, rv[0], rv[1], rv[2], rv[3], so, dst);
        dma16x4_nt(r_x, rv[0], rv[1], rv[2], rv[3], so, dst + 4096);
      }
    };
    unsigned tw[4];
    {
      const u32x4 r_xf0 = make_rsrc(xf, TENSOR_BYTES);
      const unsigned to = (unsigned)(((bimg * 128 + ty0 + (lane >> 5)) * 128 + tx0 + ((lane >> 1) & 15)) * 256 + (lane & 1) * 128);
#pragma unroll
      for (int i = 0; i < 4; ++i)
        asm volatile("buffer_load_dword %0, %1, %2, %3 offen" : "=v"(tw[i]) : "v"(to), "s"(r_xf0), "s"(i * 65536) : "memory");
    }
    BAR();
    rows(2); rows(3);
    BAR();
    rows(4);
#pragma unroll
    for (int T = 0; T < 2; ++T) {
      unsigned cv[4];
#pragma unroll
      for (int w_ = 0; w_ < 4; ++w_) { unsigned o = col_voff(T, w_); cv[w_] = o == OOB ? OOB : o - w_ * 1024; }
      const unsigned dst = lds0 + R2 + (T * 2 + 1) * 8192;
      dma16x4_nt(r_c, cv[0], cv[1], cv[2], cv[3], 0, dst);
      dma16x4_nt(r_x, cv[0], cv[1], cv[2], cv[3], 0, dst + 4096);
    }
    const u32x4 r_w2 = make_rsrc(wz2, 32768), r_wc = make_rsrc(wconv, 9 * 32768);
    const unsigned vA = (unsigned)(q * 256 + ((px ^ (q << 1)) * 16));
    const unsigned vB = (unsigned)(q * 256 + ((px ^ ((q | 4) << 1)) * 16));
    auto block = [&](u32x4 rs, int src, int dst) LAMBDA {
      dma16x4(rs, vA, vA, vB, vB, src, lds0 + dst);
      dma16x4(rs, vA, vA, vB, vB, src + 4096, lds0 + dst + 4096);
    };
    auto stage = [&](int s) LAMBDA {
#pragma unroll
      for (int di = 0; di < 3; ++di) block(r_wc, (di * 3 + step_dj(s)) * 32768 + (s & 3) * 8192, blk_off(s, di));
    };
    WAIT_VM(32); BAR();
    stage(0);
    WAIT_VM(24); BAR();
    stage(1);
    WAIT_VM(24); BAR();
    stage(2); stage(3);
    WAIT_VM(48); BAR();
    stage(4);
    WAIT_VM(24); BAR();
    stage(5);
    WAIT_VM(0); BAR();
    stage(6); stage(7);
    WAIT_VM(0); BAR();
    stage(8); stage(9);
    WAIT_VM(0); BAR();
    stage(10); stage(11);
    WAIT_VM(0); BAR();
    block(r_w2, 0, wz2_off(0)); block(r_w2, 8192, wz2_off(1)); block(r_w2, 16384, wz2_off(2)); block(r_w2, 24576, wz2_off(3));
    WAIT_VM(0); BAR();
    asm volatile("" :: "v"(tw[0]), "v"(tw[1]), "v"(tw[2]), "v"(tw[3]));
    return;
  }

  const int T = wv >> 2, w = wv & 3;
  STAMP(0);
  {
    const u32x4 r_w1 = make_rsrc(wz1, 32768);
    const int wk = 4 * wv + q;
    const int wf = (wk & 3) | (((wk >> 3) & 1) << 2);
    const unsigned wvoff = (unsigned)(wk * 256 + ((px ^ (wf << 1)) * 16));
#pragma unroll
    for (int ks = 0; ks < 4; ++ks) dma16(r_w1, wvoff, ks * 8192, lds0 + wv * 1024 + H_OFF + ks * 8192);
    const unsigned rv = row_voff(w);
#pragma unroll
    for (int j = 0; j < 2; ++j) {
      const unsigned so = row_soff(grp_row(T, j));
      const unsigned dst = lds0 + R0 + (T * 2 + j) * 8192 + w * 1024;
      dma16_nt(r_c, rv, so, dst);
      dma16_nt(r_x, rv, so, dst + 4096);
    }
  }
  STAMP(13);
  const u32x4 r_xf = make_rsrc(xf, TENSOR_BYTES);
  __amdgpu_buffer_rsrc_t r_o  = __builtin_amdgcn_make_buffer_rsrc((void*)out, 0, TENSOR_BYTES, 0x00020000);
  const int aq = (lane & 15) >> 2, ap = lane & 3;
  const int af = aq | ((q & 1) << 2);
  const unsigned ab0 = (unsigned)((8 * q + aq) * 256 + (((2 * w) ^ af) * 32) + 8 * ap);
  const unsigned aH0 = lds0 + ab0, aH1 = lds0 + (ab0 ^ 32);
  const unsigned aR0 = aH0 + R0, aR1 = aH1 + R0;
  const unsigned aS0 = aH0 + R2, aS1 = aH1 + R2;
  auto aptr = [&](int off, int t) LAMBDA -> const lds_char* {
    if (off < R0) return ldsp(t ? aH1 : aH0) + off;
    if (off < R2) return ldsp(t ? aR1 : aR0) + (off - R0);
    return ldsp(t ? aS1 : aS0) + (off - R2);
  };
  unsigned hw[6];
  {
    const unsigned hwbase = lds0 + H_OFF + (4 * w + (q >> 1)) * PLANE + (q & 1) * 8;
#pragma unroll
    for (int j = 0; j < 5; ++j) hw[j] = hwbase + (grp_row(T, j) * 18 + px + 1) * 16;
    hw[5] = hwbase + (px < 10 ? (5 * T + (px >> 1)) * 18 + ((px & 1) ? 17 : 0) : 180 + 6 * T + (px - 10)) * 16;
  }
  unsigned cb[4], cb2[4];
#pragma unroll
  for (int ks = 0; ks < 4; ++ks) { cb[ks] = lds0 + R0 + T * 16384 + px * 256 + (((4 * ks + q) ^ px) * 16); cb2[ks] = cb[ks] + 65536; }
  const unsigned xb0 = lds0 + R0 + T * 16384 + 4096 + px * 256 + (((4 * w + (q >> 1)) ^ px) * 16) + (q & 1) * 8;
  const unsigned xb1 = lds0 + R0 + T * 16384 + 4096 + px * 256 + (((4 * w + 2 + (q >> 1)) ^ px) * 16) + (q & 1) * 8;
  const unsigned xb2 = xb0 + 65536, xb3 = xb1 + 65536;
  const unsigned stw = lds0 + STATS_OFF + (w >> 1) * 3072 + ((T * 6 + (q >> 1)) * 16 + px) * 16 + (w & 1) * 8 + (q & 1) * 4;
  const unsigned str = lds0 + STATS_OFF + (T * 6 * 16 + px) * 16;

  f32x4 hin[6][2];
  bf16x8 A1f[4][2];
  bf16x8 Af[2][3][2], Bf[2][6];
  u32x4 xfr[4];
  bf16x8 Cb[2][4]; u32x2 Xb[2][2];
  auto z1_read = [&](auto kc) LAMBDA {
    constexpr int k = decltype(kc)::value;
#pragma unroll
    for (int jj = 0; jj < 2; ++jj) {
      if constexpr (k < 2) {
        Xb[jj][0] = *LDSP(const u32x2, ldsp(xb0) + k * 32768 + jj * 8192);
        Xb[jj][1] = *LDSP(const u32x2, ldsp(xb1) + k * 32768 + jj * 8192);
      } else {
        Xb[jj][0] = *LDSP(const u32x2, ldsp(xb2) + jj * 8192);
        Xb[jj][1] = *LDSP(const u32x2, ldsp(xb3) + jj * 8192);
      }
    }
#pragma unroll
    for (int ks = 0; ks < 4; ++ks)
#pragma unroll
      for (int jj = 0; jj < 2; ++jj)
        Cb[jj][ks] = k < 2 ? lds_b128(ldsp(cb[ks]) + k * 32768 + jj * 8192) : lds_b128(ldsp(cb2[ks]) + jj * 8192);
  };
  auto z1_unpack = [&](auto kc) LAMBDA {
    constexpr int k = decltype(kc)::value;
#pragma unroll
    for (int jj = 0; jj < 2; ++jj) {
      hin[2 * k + jj][0] = unpack4(Xb[jj][0]);
      hin[2 * k + jj][1] = unpack4(Xb[jj][1]);
    }
  };
  auto z1_mma1 = [&](auto kc, auto ic) LAMBDA {
    constexpr int k = decltype(kc)::value, i = decltype(ic)::value;
    constexpr int ks = i >> 2, jj = (i >> 1) & 1, t = i & 1;
    hin[2 * k + jj][t] = mfma(A1f[ks][t], Cb[jj][ks], hin[2 * k + jj][t]);
  };
  auto z1_stats = [&](auto kc) LAMBDA {
    constexpr int k = decltype(kc)::value;
    float st[4];
#pragma unroll
    for (int jj = 0; jj < 2; ++jj) {
      const int j = 2 * k + jj;
      f32x2 a = (lo2(hin[j][0]) + hi2(hin[j][0])) + (lo2(hin[j][1]) + hi2(hin[j][1]));
      f32x2 b = lo2(hin[j][0]) * lo2(hin[j][0]);
      b = fma2(hi2(hin[j][0]), hi2(hin[j][0]), b);
      b = fma2(lo2(hin[j][1]), lo2(hin[j][1]), b);
      b = fma2(hi2(hin[j][1]), hi2(hin[j][1]), b);
      st[2 * jj] = a[0] + a[1];
      st[2 * jj + 1] = b[0] + b[1];
    }
    float r = reduce4(st[0], st[1], st[2], st[3]);
    *LDSP(float, (lds_char*)(size_t)stw + 2 * k * 256) = r;
  };
  f32x4 sp0, sp1;
  const unsigned strh = str + (q >> 1) * 256;
  auto si_read = [&](auto kc) LAMBDA {
    constexpr int k = decltype(kc)::value;
    sp0 = *LDSP(const f32x4, ldsp(strh) + 2 * k * 256);
    sp1 = *LDSP(const f32x4, ldsp(strh) + 2 * k * 256 + 3072);
  };
  auto si_math = [&](auto kc, auto ngc, auto hook) LAMBDA {
    constexpr int k = decltype(kc)::value, NG = decltype(ngc)::value, NQ = 2 * NG;
    float rs2[2], nb2[2];
    {
      f32x2 m = ((lo2(sp0) + hi2(sp0)) + (lo2(sp1) + hi2(sp1))) * bc2(1.f / 128.f);
      float var = __builtin_fmaxf(__builtin_fmaf(-m[0], m[0], m[1]), 0.f);
      float r = __builtin_amdgcn_rsqf(var + 1e-5f) * -1.4426950408889634f;
      float n = -m[0] * r;
      auto a = __builtin_amdgcn_permlane32_swap(f2u(r), f2u(r), false, false);
      auto b = __builtin_amdgcn_permlane32_swap(f2u(n), f2u(n), false, false);
      rs2[0] = u2f(a[0]); rs2[1] = u2f(a[1]); nb2[0] = u2f(b[0]); nb2[1] = u2f(b[1]);
    }
    f32x2 t[2 * NQ], e[2 * NQ];
#pragma unroll
    for (int g = 0; g < NQ; ++g) {
      t[2 * g]     = fma2(lo2(hin[2 * k + (g >> 1)][g & 1]), bc2(rs2[g >> 1]), bc2(nb2[g >> 1]));
      t[2 * g + 1] = fma2(hi2(hin[2 * k + (g >> 1)][g & 1]), bc2(rs2[g >> 1]), bc2(nb2[g >> 1]));
    }
    SCHED();
    static_for<0, 4 * NQ>([&](auto ic) LAMBDA {
      constexpr int i = decltype(ic)::value;
      hook(ic);
      e[i >> 1][i & 1] = __builtin_amdgcn_exp2f(t[i >> 1][i & 1]);
      SCHED();
    });
    f32x2 P[NQ]; float rr[NQ];
#pragma unroll
    for (int g = 0; g < NQ; ++g) { f32x2 g1 = e[2 * g] + bc2(1.f); P[g] = fma2(g1, e[2 * g + 1], g1); }
#pragma unroll
    for (int g = 0; g < NQ; ++g) rr[g] = __builtin_amdgcn_rcpf(P[g][0] * P[g][1]);
#pragma unroll
    for (int g = 0; g < NQ; ++g) { t[2 * g] = fma2(t[2 * g], e[2 * g + 1], t[2 * g]); t[2 * g + 1] = fma2(t[2 * g + 1], e[2 * g], t[2 * g + 1]); }
#pragma unroll
    for (int g = 0; g < NQ; ++g) {
      f32x2 sw = __builtin_shufflevector(P[g], P[g], 1, 0);
      P[g] = sw * bc2(rr[g] * -0.6931471805599453f);
    }
#pragma unroll
    for (int g = 0; g < NQ; ++g) {
      u32x2 pk = {pk2v(t[2 * g] * P[g]), pk2v(t[2 * g + 1] * P[g])};
      *LDSP(u32x2, (lds_char*)(size_t)hw[2 * k + (g >> 1)] + 2 * (g & 1) * PLANE) = pk;
    }
  };
  {
    unsigned p0 = aH0, p1 = aH1, p2 = aR0, p3 = aR1, p4 = aS0, p5 = aS1;
    asm volatile("" :: "v"(p0), "v"(p1), "v"(p2), "v"(p3), "v"(p4), "v"(p5), "v"(hw[0]), "v"(hw[1]), "v"(hw[2]), "v"(hw[3]), "v"(hw[4]), "v"(hw[5]));
    asm volatile("" :: "v"(cb[0]), "v"(cb[1]), "v"(cb[2]), "v"(cb[3]), "v"(cb2[0]), "v"(cb2[1]), "v"(cb2[2]), "v"(cb2[3]), "v"(xb0), "v"(xb1), "v"(xb2), "v"(xb3), "v"(stw), "v"(strh));
  }
  WAIT_VM(4);
  BAR();
#pragma unroll
  for (int ks = 0; ks < 4; ++ks) {
    A1f[ks][0] = lds_tr_frag(aptr(H_OFF + ks * 8192, 0));
    A1f[ks][1] = lds_tr_frag(aptr(H_OFF + ks * 8192, 1));
  }
  WAIT_VM(0);
  BAR();
  STAMP(1);
  z1_read(IC<0>{});
  SCHED();
  z1_unpack(IC<0>{});
  static_for<0, 16>([&](auto ic) LAMBDA { z1_mma1(IC<0>{}, ic); });
  z1_stats(IC<0>{});
  SCHED();
  STAMP(2);
  BAR();
  STAMP(3);
  si_read(IC<0>{});
  z1_read(IC<1>{});
  SCHED();
  z1_unpack(IC<1>{});
  si_math(IC<0>{}, IC<2>{}, [&](auto ic) LAMBDA { z1_mma1(IC<1>{}, ic); });
  z1_stats(IC<1>{});
  SCHED();
  STAMP(4);
  BAR();
  STAMP(5);
  si_read(IC<1>{});
  z1_read(IC<2>{});
  const int lp = lane >> 2, lc = lane & 3;
  const unsigned xoff = (unsigned)(lp * 256 + (4 * w + (lc ^ ((lp >> 2) & 3))) * 16);
  const unsigned xsoff = (unsigned)(((bimg * 128 + ty0 + 4 * T) * 128 + tx0) * 256);
  const unsigned xrd0 = (unsigned)(px * 64 + (((q >> 1) ^ ((px >> 2) & 3)) * 16) + (q & 1) * 8);
  const unsigned xrd1 = (unsigned)(px * 64 + (((2 + (q >> 1)) ^ ((px >> 2) & 3)) * 16) + (q & 1) * 8);
  const unsigned xfl = lds0 + XF_OFF + wv * 2048;
  SCHED();
  z1_unpack(IC<2>{});
  si_math(IC<1>{}, IC<2>{}, [&](auto ic) LAMBDA { z1_mma1(IC<2>{}, ic); });
  z1_stats(IC<2>{});
  SCHED();
  STAMP(6);
  BAR();
  STAMP(7);
  si_read(IC<2>{});
#pragma unroll
  for (int di = 0; di < 3; ++di) {
    Af[0][di][0] = lds_tr_frag(aptr(blk_off(0, di), 0));
    Af[0][di][1] = lds_tr_frag(aptr(blk_off(0, di), 1));
  }
  SCHED();
  si_math(IC<2>{}, IC<2>{}, [&](auto ic) LAMBDA {});
  f32x4 acc[4][2];
#pragma unroll
  for (int i = 0; i < 4; ++i) { acc[i][0] = hin[i + 1][0]; acc[i][1] = hin[i + 1][1]; }
  SCHED();
  STAMP(8);
  BAR();
  STAMP(9);

  const lds_char* hb = ldsp(lds0 + H_OFF + q * PLANE + (T * 4 * 18 + px) * 16);
#pragma unroll
  for (int rr = 0; rr < 6; ++rr) Bf[0][rr] = lds_b128(hb + (rr * 18 + step_dj(0)) * 16);
  s16x4 Ah[2][3][2][2];
#pragma unroll
  for (int di = 0; di < 3; ++di)
#pragma unroll
    for (int t = 0; t < 2; ++t) {
      s16x8 v = __builtin_bit_cast(s16x8, Af[0][di][t]);
      Ah[0][di][t][0] = __builtin_shufflevector(v, v, 0, 1, 2, 3);
      Ah[0][di][t][1] = __builtin_shufflevector(v, v, 4, 5, 6, 7);
    }
  auto afrag = [&](int buf, int di, int t) LAMBDA -> bf16x8 {
    return __builtin_bit_cast(bf16x8, __builtin_shufflevector(Ah[buf][di][t][0], Ah[buf][di][t][1], 0, 1, 2, 3, 4, 5, 6, 7));
  };
  bf16x8 A2f[8];
  f32x4 o2[8];
  u32x2 xq[4][2];
  const unsigned abz = lds0 + (unsigned)((8 * q + aq) * 256 + (af * 32) + 8 * ap);
  const unsigned owbase = lds0 + (w < 3 ? R2 + w * 8192 : R1 + 8192) + (q >> 1) * HOUT_PLANE + (q & 1) * 8 + (4 * T * 16 + px) * 16;
  static_for<0, 12>([&](auto sc) LAMBDA {
    constexpr int s = decltype(sc)::value;
    constexpr int cur = s & 1, nxt = cur ^ 1;
    SCHED();
    if constexpr ((s & 1) == 1) BAR();
    SCHED();
    static_for<0, 24>([&](auto mc) LAMBDA {
      constexpr int m = decltype(mc)::value;
      constexpr int di = s < 11 ? m / 8 : (m % 6) / 2, i = s < 11 ? (m % 8) / 2 : m / 6, t = m & 1;
      acc[i][t] = mfma(afrag(cur, di, t), Bf[cur][i + di], acc[i][t]);
      if constexpr (s + 1 < 12) {
        constexpr int s1 = s + 1, dj = step_dj(s1), ks = s1 & 3;
        if constexpr (m < 12) {
          constexpr int rdi = m >> 2, rt = (m >> 1) & 1, rh = m & 1;
          Ah[nxt][rdi][rt][rh] = __builtin_amdgcn_ds_read_tr16_b64_v4i16(LDSP(s16x4, aptr(blk_off(s1, rdi), rt) + rh * 1024));
        } else if constexpr (m < 18) {
          constexpr int rr = m - 12;
          Bf[nxt][rr] = lds_b128(hb + ks * 4 * PLANE + (rr * 18 + dj) * 16);
        }
      } else {
        if constexpr (m < 8) A2f[m] = lds_tr_frag(aptr(wz2_off(m >> 1), m & 1));
      }
      if constexpr ((s == 3 || s == 7) && (m == 4 || m == 12)) {
        constexpr int r = (s == 3 ? 0 : 2) + (m == 12 ? 1 : 0);
        dma16(r_xf, xoff, xsoff + r * 32768, xfl + (r & 1) * 1024);
      }
      if constexpr ((s == 6 || s == 10) && m == 20) WAIT_VM(0);
      if constexpr ((s == 6 || s == 10) && m >= 20) {
        constexpr int r = (s == 6 ? 0 : 2) + ((m >> 1) & 1), tt = m & 1;
        xq[r][tt] = *LDSP(const u32x2, ldsp(xfl + (tt ? xrd1 : xrd0)) + (r & 1) * 1024);
      }
      if constexpr (s == 11 && m >= 8 && m < 16) { constexpr int e = m - 8; o2[e] = unpack4(xq[e >> 1][e & 1]); }
      if constexpr (s == 11 && m >= 8 && (m % 6) == 2) {
        constexpr int r = m / 6 - 1;
#pragma unroll
        for (int tt = 0; tt < 2; ++tt) {
          u32x2 pk = {pk2(acc[r][tt][0], acc[r][tt][1]), pk2(acc[r][tt][2], acc[r][tt][3])};
          *LDSP(u32x2, (lds_char*)(size_t)owbase + 2 * tt * HOUT_PLANE + r * 256) = pk;
        }
      }
      SCHED();
    });
  });
#pragma unroll
  for (int tt = 0; tt < 2; ++tt) {
    u32x2 pk = {pk2(acc[3][tt][0], acc[3][tt][1]), pk2(acc[3][tt][2], acc[3][tt][3])};
    *LDSP(u32x2, (lds_char*)(size_t)owbase + 2 * tt * HOUT_PLANE + 3 * 256) = pk;
  }
  SCHED();
  STAMP(10);
  BAR();
  STAMP(11);
  {
    bf16x8 B[4][4];
    const unsigned hr = lds0 + q * HOUT_PLANE + (4 * T * 16 + px) * 16;
#pragma unroll
    for (int i = 0; i < 4; ++i)
#pragma unroll
      for (int ks = 0; ks < 4; ++ks) B[ks][i] = lds_b128(ldsp(hr + hout_off(ks)) + i * 256);
    SCHED();
    const unsigned ost = lds0 + H_OFF + wv * 4096;
    const unsigned ow0 = ost + xrd0, ow1 = ost + xrd1, ord = ost + lane * 16;
    u32x4 ov[4];
    auto put = [&](auto ic) LAMBDA {
      constexpr int i = decltype(ic)::value;
      if constexpr (i < 4) {
        u32x2 p0 = {pk2(o2[2 * i][0], o2[2 * i][1]), pk2(o2[2 * i][2], o2[2 * i][3])};
        u32x2 p1 = {pk2(o2[2 * i + 1][0], o2[2 * i + 1][1]), pk2(o2[2 * i + 1][2], o2[2 * i + 1][3])};
        *LDSP(u32x2, (lds_char*)(size_t)ow0 + i * 1024) = p0;
        *LDSP(u32x2, (lds_char*)(size_t)ow1 + i * 1024) = p1;
        ov[i] = *LDSP(const u32x4, ldsp(ord) + i * 1024);
      }
    };
    static_for<0, 4>([&](auto ic) LAMBDA {
      constexpr int i = decltype(ic)::value;
      static_for<0, 4>([&](auto kc) LAMBDA {
        constexpr int ks = decltype(kc)::value;
        o2[2 * i]     = mfma(A2f[2 * ks],     B[ks][i], o2[2 * i]);
        o2[2 * i + 1] = mfma(A2f[2 * ks + 1], B[ks][i], o2[2 * i + 1]);
        if constexpr (ks == 1) put(IC<(i > 0 ? i - 1 : 4)>{});
        if constexpr (ks == 0) { if constexpr (decltype(ic)::value > 1) __builtin_amdgcn_raw_buffer_store_b128(ov[(i > 1 ? i - 2 : 0)], r_o, xoff, xsoff + (i - 2) * 32768, STORE_AUX); }
        SCHED();
      });
    });
    __builtin_amdgcn_raw_buffer_store_b128(ov[2], r_o, xoff, xsoff + 2 * 32768, STORE_AUX);
    put(IC<3>{});
    __builtin_amdgcn_raw_buffer_store_b128(ov[3], r_o, xoff, xsoff + 3 * 32768, STORE_AUX);
  }
  STAMP(12);
}

extern "C" void kernel_launch(void* const* d_in, const int* in_sizes, int n_in,
                              void* d_out, int out_size, void* d_ws, size_t ws_size,
                              hipStream_t stream) {
  const __hip_bfloat16* xf  = (const __hip_bfloat16*)d_in[0];
  const __hip_bfloat16* x   = (const __hip_bfloat16*)d_in[1];
  const __hip_bfloat16* c   = (const __hip_bfloat16*)d_in[2];
  const __hip_bfloat16* wz1 = (const __hip_bfloat16*)d_in[3];
  const __hip_bfloat16* wz2 = (const __hip_bfloat16*)d_in[4];
  const __hip_bfloat16* wc  = (const __hip_bfloat16*)d_in[5];
  (void)hipFuncSetAttribute((const void*)controlnet_fused, hipFuncAttributeMaxDynamicSharedMemorySize, LDS_BYTES);
  controlnet_fused<<<256, NTHREADS, LDS_BYTES, stream>>>(xf, x, c, wz1, wz2, wc, (__hip_bfloat16*)d_out
  );
}
```

```cpp
#include <hip/hip_runtime.h>
#include <hip/hip_bf16.h>

typedef __attribute__((ext_vector_type(8))) __bf16 bf16x8;
typedef __attribute__((ext_vector_type(2))) __bf16 bf16x2;
typedef __attribute__((ext_vector_type(4))) short s16x4;
typedef __attribute__((ext_vector_type(8))) short s16x8;
typedef __attribute__((ext_vector_type(4))) float f32x4;
typedef __attribute__((ext_vector_type(2))) float f32x2;
typedef __attribute__((ext_vector_type(4))) unsigned u32x4;
typedef __attribute__((ext_vector_type(2))) unsigned u32x2;
typedef __attribute__((address_space(3))) char lds_char;
#define LDSP(T, p) ((__attribute__((address_space(3))) T*)(p))
#define DEVINL __device__ __forceinline__
#define LAMBDA __attribute__((always_inline))

constexpr int PLANE     = 3072;
constexpr int H_OFF     = 0;
constexpr int R0 = 49152, R1 = 81920, R2 = 114688;
constexpr int STATS_OFF = 147456;
constexpr int XF_OFF    = STATS_OFF;
constexpr int LDS_BYTES = STATS_OFF + 16384;
constexpr int HOUT_PLANE = 2048;
__host__ __device__ constexpr int reg_off(int a) { return a == 0 ? R0 : a == 1 ? R1 : R2; }
__host__ __device__ constexpr int blk_off(int s, int di) { return (s & 3) < 3 ? reg_off(s & 3) + di * 8192 : reg_off(di) + 24576; }
__host__ __device__ constexpr int wz2_off(int ks) { return ks < 3 ? R0 + ks * 8192 : R1; }
__host__ __device__ constexpr int hout_off(int ks) { return ks < 3 ? R2 + ks * 8192 : R1 + 8192; }
constexpr unsigned OOB  = 0x40000000u;
constexpr int TENSOR_BYTES = 2 * 128 * 128 * 128 * 2;
constexpr int NTHREADS = 576;

#define STAMP(i) do {} while (0)
#define STORE_AUX 17
#define WAIT_VM(n) asm volatile("s_waitcnt vmcnt(" #n ")" ::: "memory")
#define BAR() asm volatile("s_waitcnt lgkmcnt(0)\n\ts_barrier" ::: "memory")
#define SCHED() __builtin_amdgcn_sched_barrier(0)

template <int N> struct IC { static constexpr int value = N; };
template <int I, int N, class F> DEVINL void static_for(F&& f) { if constexpr (I < N) { f(IC<I>{}); static_for<I + 1, N>(f); } }

DEVINL u32x4 make_rsrc(const void* p, unsigned bytes) {
  unsigned long long a = (unsigned long long)p;
  u32x4 r = {(unsigned)a, (unsigned)(a >> 32) & 0xffffu, bytes, 0x00020000u};
  return r;
}
DEVINL void dma16(u32x4 rsrc, unsigned voff, unsigned soff, unsigned lds_addr) {
  asm volatile("s_mov_b32 m0, %0\n\ts_nop 0\n\tbuffer_load_dwordx4 %1, %2, %3 offen lds" :: "s"(lds_addr), "v"(voff), "s"(rsrc), "s"(soff) : "memory");
}
DEVINL void dma16_nt(u32x4 rsrc, unsigned voff, unsigned soff, unsigned lds_addr) {
  asm volatile("s_mov_b32 m0, %0\n\ts_nop 0\n\tbuffer_load_dwordx4 %1, %2, %3 offen nt lds" :: "s"(lds_addr), "v"(voff), "s"(rsrc), "s"(soff) : "memory");
}
DEVINL void dma16x4(u32x4 rsrc, unsigned v0, unsigned v1, unsigned v2, unsigned v3, unsigned soff, unsigned lds_addr) {
  asm volatile("s_mov_b32 m0, %0\n\ts_nop 0\n\t"
               "buffer_load_dwordx4 %1, %5, %6 offen lds\n\t"
               "buffer_load_dwordx4 %2, %5, %6 offen offset:1024 lds\n\t"
               "buffer_load_dwordx4 %3, %5, %6 offen offset:2048 lds\n\t"
               "buffer_load_dwordx4 %4, %5, %6 offen offset:3072 lds"
               :: "s"(lds_addr), "v"(v0), "v"(v1), "v"(v2), "v"(v3), "s"(rsrc), "s"(soff) : "memory");
}
DEVINL void dma16x4_nt(u32x4 rsrc, unsigned v0, unsigned v1, unsigned v2, unsigned v3, unsigned soff, unsigned lds_addr) {
  asm volatile("s_mov_b32 m0, %0\n\ts_nop 0\n\t"
               "buffer_load_dwordx4 %1, %5, %6 offen nt lds\n\t"
               "buffer_load_dwordx4 %2, %5, %6 offen offset:1024 nt lds\n\t"
               "buffer_load_dwordx4 %3, %5, %6 offen offset:2048 nt lds\n\t"
               "buffer_load_dwordx4 %4, %5, %6 offen offset:3072 nt lds"
               :: "s"(lds_addr), "v"(v0), "v"(v1), "v"(v2), "v"(v3), "s"(rsrc), "s"(soff) : "memory");
}
DEVINL const lds_char* ldsp(unsigned a) { return (const lds_char*)(size_t)a; }
DEVINL lds_char* ldsw(unsigned a) { return (lds_char*)(size_t)a; }
DEVINL bf16x8 lds_b128(const lds_char* p) { return *LDSP(const bf16x8, p); }
DEVINL s16x4 lds_tr(const lds_char* p) { return __builtin_amdgcn_ds_read_tr16_b64_v4i16(LDSP(s16x4, p)); }
DEVINL bf16x8 join(s16x4 lo, s16x4 hi) { return __builtin_bit_cast(bf16x8, __builtin_shufflevector(lo, hi, 0, 1, 2, 3, 4, 5, 6, 7)); }
DEVINL bf16x8 lds_tr_frag(const lds_char* p) { return join(lds_tr(p), lds_tr(p + 1024)); }
DEVINL f32x4 mfma(bf16x8 a, bf16x8 b, f32x4 c) { return __builtin_amdgcn_mfma_f32_16x16x32_bf16(a, b, c, 0, 0, 0); }
DEVINL unsigned pk2v(f32x2 f) { return __builtin_bit_cast(unsigned, __builtin_convertvector(f, bf16x2)); }
DEVINL f32x2 lo2(f32x4 v) { return __builtin_shufflevector(v, v, 0, 1); }
DEVINL f32x2 hi2(f32x4 v) { return __builtin_shufflevector(v, v, 2, 3); }
DEVINL u32x2 pack4(f32x4 v) { u32x2 r = {pk2v(lo2(v)), pk2v(hi2(v))}; return r; }
DEVINL float bf_lo(unsigned u) { return __builtin_bit_cast(float, u << 16); }
DEVINL float bf_hi(unsigned u) { return __builtin_bit_cast(float, u & 0xffff0000u); }
DEVINL unsigned f2u(float f) { return __builtin_bit_cast(unsigned, f); }
DEVINL float u2f(unsigned u) { return __builtin_bit_cast(float, u); }
DEVINL f32x4 unpack4(u32x2 v) { f32x4 r = {bf_lo(v[0]), bf_hi(v[0]), bf_lo(v[1]), bf_hi(v[1])}; return r; }
DEVINL f32x2 bc2(float v) { f32x2 r = {v, v}; return r; }
DEVINL f32x2 fma2(f32x2 a, f32x2 b, f32x2 c) { return __builtin_elementwise_fma(a, b, c); }
DEVINL float reduce4(float a, float b, float c, float d) {
  auto r0 = __builtin_amdgcn_permlane16_swap(f2u(a), f2u(b), false, false);
  auto r1 = __builtin_amdgcn_permlane16_swap(f2u(c), f2u(d), false, false);
  float ab = u2f(r0[0]) + u2f(r0[1]);
  float cd = u2f(r1[0]) + u2f(r1[1]);
  auto r2 = __builtin_amdgcn_permlane32_swap(f2u(ab), f2u(cd), false, false);
  return u2f(r2[0]) + u2f(r2[1]);
}

__global__ __launch_bounds__(NTHREADS, 3)
void controlnet_fused(const __hip_bfloat16* __restrict__ xf, const __hip_bfloat16* __restrict__ x,
                      const __hip_bfloat16* __restrict__ c, const __hip_bfloat16* __restrict__ wz1,
                      const __hip_bfloat16* __restrict__ wz2, const __hip_bfloat16* __restrict__ wconv,
                      __hip_bfloat16* __restrict__ out
                      ) {
  extern __shared__ __attribute__((aligned(16))) char smem_generic[];
  const unsigned lds0 = (unsigned)(size_t)(lds_char*)smem_generic;

  const int tid = threadIdx.x;
  const int lane = tid & 63;
  const int wv = __builtin_amdgcn_readfirstlane(tid >> 6);
  const int q = lane >> 4, px = lane & 15;
  const int bid = blockIdx.x;
  const int tile = (bid & 7) * 32 + (bid >> 3);
  const int bimg = tile >> 7, ty0 = ((tile >> 3) & 15) * 8, tx0 = (tile & 7) * 16;
  const u32x4 r_c  = make_rsrc(c, TENSOR_BYTES), r_x = make_rsrc(x, TENSOR_BYTES), r_xf = make_rsrc(xf, TENSOR_BYTES);

  auto grp_row = [&](int T, int j) LAMBDA -> int { return T == 0 ? j : (j == 0 ? 9 : j + 4); };
  auto row_soff = [&](int r) LAMBDA -> unsigned {
    const int gy = ty0 - 1 + r;
    return (unsigned)gy < 128u ? (unsigned)(((bimg * 128 + gy) * 128 + tx0) * 256) : OOB;
  };
  auto row_voff = [&](int w_) LAMBDA -> unsigned { const int p = 4 * w_ + q; return (unsigned)(p * 256 + ((px ^ p) * 16)); };
  auto col_voff = [&](int T, int w_) LAMBDA -> unsigned {
    const int p = 4 * w_ + q;
    const int gy = ty0 - 1 + 5 * T + (p >> 1), gx = tx0 - 1 + ((p & 1) ? 17 : 0);
    const bool ok = p < 10 && (unsigned)gy < 128u && (unsigned)gx < 128u;
    return ok ? (unsigned)(((bimg * 128 + gy) * 128 + gx) * 256 + ((px ^ p) * 16)) : OOB;
  };

  if (wv == 8) {
    unsigned rv[4];
#pragma unroll
    for (int w_ = 0; w_ < 4; ++w_) rv[w_] = row_voff(w_) - w_ * 1024;
    auto rows = [&](int j) LAMBDA {
#pragma unroll
      for (int T = 0; T < 2; ++T) {
        const unsigned so = row_soff(grp_row(T, j));
        const unsigned dst = lds0 + reg_off(j >> 1) + (T * 2 + (j & 1)) * 8192;
        dma16x4_nt(r_c, rv[0], rv[1], rv[2], rv[3], so, dst);
        dma16x4_nt(r_x, rv[0], rv[1], rv[2], rv[3], so, dst + 4096);
      }
    };
    unsigned tw[4];
    {
      const unsigned to = (unsigned)(((bimg * 128 + ty0 + (lane >> 5)) * 128 + tx0 + ((lane >> 1) & 15)) * 256 + (lane & 1) * 128);
#pragma unroll
      for (int i = 0; i < 4; ++i)
        asm volatile("buffer_load_dword %0, %1, %2, %3 offen" : "=v"(tw[i]) : "v"(to), "s"(r_xf), "s"(i * 65536) : "memory");
    }
    BAR();
    rows(2); rows(3);
    BAR();
    rows(4);
#pragma unroll
    for (int T = 0; T < 2; ++T) {
      unsigned cv[4];
#pragma unroll
      for (int w_ = 0; w_ < 4; ++w_) { unsigned o = col_voff(T, w_); cv[w_] = o == OOB ? OOB : o - w_ * 1024; }
      const unsigned dst = lds0 + R2 + (T * 2 + 1) * 8192;
      dma16x4_nt(r_c, cv[0], cv[1], cv[2], cv[3], 0, dst);
      dma16x4_nt(r_x, cv[0], cv[1], cv[2], cv[3], 0, dst + 4096);
    }
    const u32x4 r_w2 = make_rsrc(wz2, 32768), r_wc = make_rsrc(wconv, 9 * 32768);
    const unsigned vA = (unsigned)(q * 256 + ((px ^ (q << 1)) * 16));
    const unsigned vB = (unsigned)(q * 256 + ((px ^ ((q | 4) << 1)) * 16));
    auto block = [&](u32x4 rs, int src, int dst) LAMBDA {
      dma16x4(rs, vA, vA, vB, vB, src, lds0 + dst);
      dma16x4(rs, vA, vA, vB, vB, src + 4096, lds0 + dst + 4096);
    };
    auto stage = [&](int s) LAMBDA {
#pragma unroll
      for (int di = 0; di < 3; ++di) block(r_wc, (di * 3 + (s >> 2)) * 32768 + (s & 3) * 8192, blk_off(s, di));
    };
    WAIT_VM(32); BAR();
    stage(0);
    WAIT_VM(24); BAR();
    stage(1);
    WAIT_VM(24); BAR();
    stage(2); stage(3);
    WAIT_VM(48); BAR();
    stage(4);
    WAIT_VM(24); BAR();
    stage(5);
    WAIT_VM(0); BAR();
    stage(6); stage(7);
    WAIT_VM(0); BAR();
    stage(8); stage(9);
    WAIT_VM(0); BAR();
    stage(10); stage(11);
    WAIT_VM(0); BAR();
    block(r_w2, 0, wz2_off(0)); block(r_w2, 8192, wz2_off(1)); block(r_w2, 16384, wz2_off(2)); block(r_w2, 24576, wz2_off(3));
    WAIT_VM(0); BAR();
    asm volatile("" :: "v"(tw[0]), "v"(tw[1]), "v"(tw[2]), "v"(tw[3]));
    return;
  }

  const int T = wv >> 2, w = wv & 3;
  STAMP(0);
  {
    const u32x4 r_w1 = make_rsrc(wz1, 32768);
    const int wk = 4 * wv + q;
    const int wf = (wk & 3) | (((wk >> 3) & 1) << 2);
    const unsigned wvoff = (unsigned)(wk * 256 + ((px ^ (wf << 1)) * 16));
#pragma unroll
    for (int ks = 0; ks < 4; ++ks) dma16(r_w1, wvoff, ks * 8192, lds0 + wv * 1024 + H_OFF + ks * 8192);
    const unsigned rv = row_voff(w);
#pragma unroll
    for (int j = 0; j < 2; ++j) {
      const unsigned so = row_soff(grp_row(T, j));
      const unsigned dst = lds0 + R0 + (T * 2 + j) * 8192 + w * 1024;
      dma16_nt(r_c, rv, so, dst);
      dma16_nt(r_x, rv, so, dst + 4096);
    }
  }
  __amdgpu_buffer_rsrc_t r_o = __builtin_amdgcn_make_buffer_rsrc((void*)out, 0, TENSOR_BYTES, 0x00020000);
  const int aq = (lane & 15) >> 2, ap = lane & 3;
  const int af = aq | ((q & 1) << 2);
  const unsigned ab0 = (unsigned)((8 * q + aq) * 256 + (((2 * w) ^ af) * 32) + 8 * ap);
  const unsigned aH0 = lds0 + ab0, aH1 = lds0 + (ab0 ^ 32);
  const unsigned aR0 = aH0 + R0, aR1 = aH1 + R0;
  const unsigned aS0 = aH0 + R2, aS1 = aH1 + R2;
  auto aptr = [&](int off, int t) LAMBDA -> const lds_char* {
    if (off < R0) return ldsp(t ? aH1 : aH0) + off;
    if (off < R2) return ldsp(t ? aR1 : aR0) + (off - R0);
    return ldsp(t ? aS1 : aS0) + (off - R2);
  };
  unsigned hw[6];
  {
    const unsigned hwbase = lds0 + H_OFF + (4 * w + (q >> 1)) * PLANE + (q & 1) * 8;
#pragma unroll
    for (int j = 0; j < 5; ++j) hw[j] = hwbase + (grp_row(T, j) * 18 + px + 1) * 16;
    hw[5] = hwbase + (px < 10 ? (5 * T + (px >> 1)) * 18 + ((px & 1) ? 17 : 0) : 180 + 6 * T + (px - 10)) * 16;
  }
  unsigned cb[4], cb2[4];
#pragma unroll
  for (int ks = 0; ks < 4; ++ks) { cb[ks] = lds0 + R0 + T * 16384 + px * 256 + (((4 * ks + q) ^ px) * 16); cb2[ks] = cb[ks] + 65536; }
  const unsigned xb0 = lds0 + R0 + T * 16384 + 4096 + px * 256 + (((4 * w + (q >> 1)) ^ px) * 16) + (q & 1) * 8;
  const unsigned xb1 = lds0 + R0 + T * 16384 + 4096 + px * 256 + (((4 * w + 2 + (q >> 1)) ^ px) * 16) + (q & 1) * 8;
  const unsigned xb2 = xb0 + 65536, xb3 = xb1 + 65536;
  const unsigned stw = lds0 + STATS_OFF + (w >> 1) * 3072 + ((T * 6 + (q >> 1)) * 16 + px) * 16 + (w & 1) * 8 + (q & 1) * 4;
  const unsigned strh = lds0 + STATS_OFF + ((T * 6 + (q >> 1)) * 16 + px) * 16;

  f32x4 hin[6][2];
  bf16x8 A1f[4][2];
  bf16x8 Cb[2][4]; u32x2 Xb[2][2];
  auto z1_read = [&](auto kc) LAMBDA {
    constexpr int k = decltype(kc)::value;
#pragma unroll
    for (int jj = 0; jj < 2; ++jj) {
      Xb[jj][0] = *LDSP(const u32x2, ldsp(k < 2 ? xb0 : xb2) + (k & 1) * 32768 + jj * 8192);
      Xb[jj][1] = *LDSP(const u32x2, ldsp(k < 2 ? xb1 : xb3) + (k & 1) * 32768 + jj * 8192);
    }
#pragma unroll
    for (int ks = 0; ks < 4; ++ks)
#pragma unroll
      for (int jj = 0; jj < 2; ++jj) Cb[jj][ks] = lds_b128(ldsp(k < 2 ? cb[ks] : cb2[ks]) + (k & 1) * 32768 + jj * 8192);
  };
  auto z1_unpack = [&](auto kc) LAMBDA {
    constexpr int k = decltype(kc)::value;
#pragma unroll
    for (int jj = 0; jj < 2; ++jj) {
      hin[2 * k + jj][0] = unpack4(Xb[jj][0]);
      hin[2 * k + jj][1] = unpack4(Xb[jj][1]);
    }
  };
  auto z1_mma1 = [&](auto kc, auto ic) LAMBDA {
    constexpr int k = decltype(kc)::value, i = decltype(ic)::value;
    constexpr int ks = i >> 2, jj = (i >> 1) & 1, t = i & 1;
    hin[2 * k + jj][t] = mfma(A1f[ks][t], Cb[jj][ks], hin[2 * k + jj][t]);
  };
  auto z1_stats = [&](auto kc) LAMBDA {
    constexpr int k = decltype(kc)::value;
    float st[4];
#pragma unroll
    for (int jj = 0; jj < 2; ++jj) {
      const int j = 2 * k + jj;
      f32x2 a = (lo2(hin[j][0]) + hi2(hin[j][0])) + (lo2(hin[j][1]) + hi2(hin[j][1]));
      f32x2 b = lo2(hin[j][0]) * lo2(hin[j][0]);
      b = fma2(hi2(hin[j][0]), hi2(hin[j][0]), b);
      b = fma2(lo2(hin[j][1]), lo2(hin[j][1]), b);
      b = fma2(hi2(hin[j][1]), hi2(hin[j][1]), b);
      st[2 * jj] = a[0] + a[1];
      st[2 * jj + 1] = b[0] + b[1];
    }
    float r = reduce4(st[0], st[1], st[2], st[3]);
    *LDSP(float, ldsw(stw) + 2 * k * 256) = r;
  };
  f32x4 sp0, sp1;
  auto si_read = [&](auto kc) LAMBDA {
    constexpr int k = decltype(kc)::value;
    sp0 = *LDSP(const f32x4, ldsp(strh) + 2 * k * 256);
    sp1 = *LDSP(const f32x4, ldsp(strh) + 2 * k * 256 + 3072);
  };
  auto si_math = [&](auto kc, auto hook) LAMBDA {
    constexpr int k = decltype(kc)::value;
    float rs2[2], nb2[2];
    {
      f32x2 m = ((lo2(sp0) + hi2(sp0)) + (lo2(sp1) + hi2(sp1))) * bc2(1.f / 128.f);
      float var = __builtin_fmaxf(__builtin_fmaf(-m[0], m[0], m[1]), m[1] * 1e-6f);
      float r = __builtin_amdgcn_rsqf(var + 1e-5f) * -1.4426950408889634f;
      float n = -m[0] * r;
      auto a = __builtin_amdgcn_permlane32_swap(f2u(r), f2u(r), false, false);
      auto b = __builtin_amdgcn_permlane32_swap(f2u(n), f2u(n), false, false);
      rs2[0] = u2f(a[0]); rs2[1] = u2f(a[1]); nb2[0] = u2f(b[0]); nb2[1] = u2f(b[1]);
    }
    f32x2 t[8], e[8];
#pragma unroll
    for (int g = 0; g < 4; ++g) {
      t[2 * g]     = fma2(lo2(hin[2 * k + (g >> 1)][g & 1]), bc2(rs2[g >> 1]), bc2(nb2[g >> 1]));
      t[2 * g + 1] = fma2(hi2(hin[2 * k + (g >> 1)][g & 1]), bc2(rs2[g >> 1]), bc2(nb2[g >> 1]));
    }
    SCHED();
    static_for<0, 16>([&](auto ic) LAMBDA {
      constexpr int i = decltype(ic)::value;
      hook(ic);
      e[i >> 1][i & 1] = __builtin_amdgcn_exp2f(t[i >> 1][i & 1]);
      SCHED();
    });
    f32x2 P[4]; float rr[4];
#pragma unroll
    for (int g = 0; g < 4; ++g) { f32x2 g1 = e[2 * g] + bc2(1.f); P[g] = fma2(g1, e[2 * g + 1], g1); }
#pragma unroll
    for (int g = 0; g < 4; ++g) rr[g] = __builtin_amdgcn_rcpf(P[g][0] * P[g][1]);
#pragma unroll
    for (int g = 0; g < 4; ++g) { t[2 * g] = fma2(t[2 * g], e[2 * g + 1], t[2 * g]); t[2 * g + 1] = fma2(t[2 * g + 1], e[2 * g], t[2 * g + 1]); }
#pragma unroll
    for (int g = 0; g < 4; ++g) {
      f32x2 sw = __builtin_shufflevector(P[g], P[g], 1, 0);
      P[g] = sw * bc2(rr[g] * -0.6931471805599453f);
    }
#pragma unroll
    for (int g = 0; g < 4; ++g) {
      u32x2 pk = {pk2v(t[2 * g] * P[g]), pk2v(t[2 * g + 1] * P[g])};
      *LDSP(u32x2, ldsw(hw[2 * k + (g >> 1)]) + 2 * (g & 1) * PLANE) = pk;
    }
  };

  asm volatile("" :: "v"(aH0), "v"(aH1), "v"(aR0), "v"(aR1), "v"(aS0), "v"(aS1), "v"(hw[0]), "v"(hw[1]), "v"(hw[2]), "v"(hw[3]), "v"(hw[4]), "v"(hw[5]));
  asm volatile("" :: "v"(cb[0]), "v"(cb[1]), "v"(cb[2]), "v"(cb[3]), "v"(cb2[0]), "v"(cb2[1]), "v"(cb2[2]), "v"(cb2[3]), "v"(xb0), "v"(xb1), "v"(xb2), "v"(xb3), "v"(stw), "v"(strh));
  WAIT_VM(4);
  BAR();
#pragma unroll
  for (int ks = 0; ks < 4; ++ks) {
    A1f[ks][0] = lds_tr_frag(aptr(H_OFF + ks * 8192, 0));
    A1f[ks][1] = lds_tr_frag(aptr(H_OFF + ks * 8192, 1));
  }
  WAIT_VM(0);
  BAR();
  STAMP(1);
  z1_read(IC<0>{});
  SCHED();
  z1_unpack(IC<0>{});
  static_for<0, 16>([&](auto ic) LAMBDA { z1_mma1(IC<0>{}, ic); });
  z1_stats(IC<0>{});
  SCHED();
  BAR();
  STAMP(3);
  si_read(IC<0>{});
  z1_read(IC<1>{});
  SCHED();
  z1_unpack(IC<1>{});
  si_math(IC<0>{}, [&](auto ic) LAMBDA { z1_mma1(IC<1>{}, ic); });
  z1_stats(IC<1>{});
  SCHED();
  BAR();
  STAMP(5);
  si_read(IC<1>{});
  z1_read(IC<2>{});
  SCHED();
  z1_unpack(IC<2>{});
  si_math(IC<1>{}, [&](auto ic) LAMBDA { z1_mma1(IC<2>{}, ic); });
  z1_stats(IC<2>{});
  SCHED();
  BAR();
  STAMP(7);
  si_read(IC<2>{});
  s16x4 Ah[2][3][2][2];
#pragma unroll
  for (int di = 0; di < 3; ++di)
#pragma unroll
    for (int t = 0; t < 2; ++t) {
      Ah[0][di][t][0] = lds_tr(aptr(blk_off(0, di), t));
      Ah[0][di][t][1] = lds_tr(aptr(blk_off(0, di), t) + 1024);
    }
  SCHED();
  si_math(IC<2>{}, [&](auto) LAMBDA {});
  f32x4 acc[4][2];
#pragma unroll
  for (int i = 0; i < 4; ++i) { acc[i][0] = hin[i + 1][0]; acc[i][1] = hin[i + 1][1]; }
  SCHED();
  BAR();
  STAMP(9);

  const lds_char* hb = ldsp(lds0 + H_OFF + q * PLANE + (T * 4 * 18 + px) * 16);
  bf16x8 Bf[2][6];
#pragma unroll
  for (int rr = 0; rr < 6; ++rr) Bf[0][rr] = lds_b128(hb + rr * 18 * 16);
  const int lp = lane >> 2, lc = lane & 3;
  const unsigned xoff = (unsigned)(lp * 256 + (4 * w + (lc ^ ((lp >> 2) & 3))) * 16);
  const unsigned xsoff = (unsigned)(((bimg * 128 + ty0 + 4 * T) * 128 + tx0) * 256);
  const unsigned xrd0 = (unsigned)(px * 64 + (((q >> 1) ^ ((px >> 2) & 3)) * 16) + (q & 1) * 8);
  const unsigned xrd1 = (unsigned)(px * 64 + (((2 + (q >> 1)) ^ ((px >> 2) & 3)) * 16) + (q & 1) * 8);
  const unsigned xfl = lds0 + XF_OFF + wv * 2048;
  bf16x8 A2f[8], B2[4][2];
  f32x4 o2[4][2];
  u32x2 xq[4][2];
  const unsigned owbase = lds0 + (w < 3 ? R2 + w * 8192 : R1 + 8192) + (q >> 1) * HOUT_PLANE + (q & 1) * 8 + (4 * T * 16 + px) * 16;
  const unsigned hr = lds0 + q * HOUT_PLANE + (4 * T * 16 + px) * 16;
  const unsigned hr2 = hr + R2, hr1 = hr + R1 + 8192;
  auto put_hout = [&](auto rc) LAMBDA {
    constexpr int r = decltype(rc)::value;
    *LDSP(u32x2, ldsw(owbase) + r * 256) = pack4(acc[r][0]);
    *LDSP(u32x2, ldsw(owbase) + 2 * HOUT_PLANE + r * 256) = pack4(acc[r][1]);
  };
  auto b2 = [&](int ks, int i) LAMBDA -> bf16x8 { return lds_b128((ks < 3 ? ldsp(hr2) + ks * 8192 : ldsp(hr1)) + i * 256); };
  static_for<0, 12>([&](auto sc) LAMBDA {
    constexpr int s = decltype(sc)::value;
    constexpr int cur = s & 1, nxt = cur ^ 1;
    SCHED();
    if constexpr ((s & 1) == 1) BAR();
    SCHED();
    static_for<0, 24>([&](auto mc) LAMBDA {
      constexpr int m = decltype(mc)::value;
      constexpr int di = s < 11 ? m / 8 : (m % 6) / 2, i = s < 11 ? (m % 8) / 2 : m / 6, t = m & 1;
      acc[i][t] = mfma(join(Ah[cur][di][t][0], Ah[cur][di][t][1]), Bf[cur][i + di], acc[i][t]);
      if constexpr (s < 11) {
        constexpr int s1 = s + 1, dj = s1 >> 2, ks = s1 & 3;
        if constexpr (m < 12) {
          constexpr int rdi = m >> 2, rt = (m >> 1) & 1, rh = m & 1;
          Ah[nxt][rdi][rt][rh] = lds_tr(aptr(blk_off(s1, rdi), rt) + rh * 1024);
        } else if constexpr (m < 18) {
          constexpr int rr = m - 12;
          Bf[nxt][rr] = lds_b128(hb + ks * 4 * PLANE + (rr * 18 + dj) * 16);
        }
      }
      if constexpr ((s == 3 || s == 7) && (m == 4 || m == 12)) {
        constexpr int r = (s == 3 ? 0 : 2) + (m == 12 ? 1 : 0);
        dma16(r_xf, xoff, xsoff + r * 32768, xfl + (r & 1) * 1024);
      }
      if constexpr ((s == 6 || s == 10) && m == 20) WAIT_VM(0);
      if constexpr ((s == 6 || s == 10) && m >= 20) {
        constexpr int r = (s == 6 ? 0 : 2) + ((m >> 1) & 1), tt = m & 1;
        xq[r][tt] = *LDSP(const u32x2, ldsp(xfl + (tt ? xrd1 : xrd0)) + (r & 1) * 1024);
      }
      if constexpr (s == 11) {
        if constexpr (m < 8) A2f[m] = lds_tr_frag(aptr(wz2_off(m >> 1), m & 1));
        if constexpr (m >= 8 && m < 16) { constexpr int e = m - 8; o2[e >> 1][e & 1] = unpack4(xq[e >> 1][e & 1]); }
        if constexpr (m == 8) put_hout(IC<0>{});
        if constexpr (m == 14) put_hout(IC<1>{});
        if constexpr (m == 16) BAR();
        if constexpr (m >= 16) { constexpr int e = m - 16; B2[e & 3][e >> 2] = b2(e & 3, e >> 2); }
        if constexpr (m == 20) put_hout(IC<2>{});
      }
      SCHED();
    });
  });
  put_hout(IC<3>{});
  SCHED();
  STAMP(10);
  BAR();
  STAMP(11);
  {
    bf16x8 B3[4][2];
#pragma unroll
    for (int i = 0; i < 2; ++i)
#pragma unroll
      for (int ks = 0; ks < 4; ++ks) B3[ks][i] = b2(ks, 2 + i);
    SCHED();
    const unsigned ost = lds0 + H_OFF + wv * 4096;
    const unsigned ow0 = ost + xrd0, ow1 = ost + xrd1, ord = ost + lane * 16;
    u32x4 ov[4];
    auto put = [&](auto ic) LAMBDA {
      constexpr int i = decltype(ic)::value;
      *LDSP(u32x2, ldsw(ow0) + i * 1024) = pack4(o2[i][0]);
      *LDSP(u32x2, ldsw(ow1) + i * 1024) = pack4(o2[i][1]);
      ov[i] = *LDSP(const u32x4, ldsp(ord) + i * 1024);
    };
    auto store = [&](auto ic) LAMBDA {
      constexpr int i = decltype(ic)::value;
      __builtin_amdgcn_raw_buffer_store_b128(ov[i], r_o, xoff, xsoff + i * 32768, STORE_AUX);
    };
    static_for<0, 16>([&](auto mc) LAMBDA {
      constexpr int m = decltype(mc)::value, i = m >> 2, ks = m & 3;
      bf16x8 B;
      if constexpr (i < 2) B = B2[ks][i]; else B = B3[ks][i - 2];
      o2[i][0] = mfma(A2f[2 * ks],     B, o2[i][0]);
      o2[i][1] = mfma(A2f[2 * ks + 1], B, o2[i][1]);
      if constexpr (m == 5) put(IC<0>{});
      if constexpr (m == 8) store(IC<0>{});
      if constexpr (m == 9) put(IC<1>{});
      if constexpr (m == 12) store(IC<1>{});
      if constexpr (m == 13) put(IC<2>{});
      SCHED();
    });
    store(IC<2>{});
    put(IC<3>{});
    store(IC<3>{});
  }
  STAMP(12);
}

extern "C" void kernel_launch(void* const* d_in, const int* in_sizes, int n_in,
                              void* d_out, int out_size, void* d_ws, size_t ws_size,
                              hipStream_t stream) {
  const __hip_bfloat16* xf  = (const __hip_bfloat16*)d_in[0];
  const __hip_bfloat16* x   = (const __hip_bfloat16*)d_in[1];
  const __hip_bfloat16* c   = (const __hip_bfloat16*)d_in[2];
  const __hip_bfloat16* wz1 = (const __hip_bfloat16*)d_in[3];
  const __hip_bfloat16* wz2 = (const __hip_bfloat16*)d_in[4];
  const __hip_bfloat16* wc  = (const __hip_bfloat16*)d_in[5];
  (void)hipFuncSetAttribute((const void*)controlnet_fused, hipFuncAttributeMaxDynamicSharedMemorySize, LDS_BYTES);
  controlnet_fused<<<256, NTHREADS, LDS_BYTES, stream>>>(xf, x, c, wz1, wz2, wc, (__hip_bfloat16*)d_out
  );
}
```

```cpp
#include <hip/hip_runtime.h>
#include <hip/hip_bf16.h>

typedef __attribute__((ext_vector_type(8))) __bf16 bf16x8;
typedef __attribute__((ext_vector_type(2))) __bf16 bf16x2;
typedef __attribute__((ext_vector_type(4))) short s16x4;
typedef __attribute__((ext_vector_type(8))) short s16x8;
typedef __attribute__((ext_vector_type(4))) float f32x4;
typedef __attribute__((ext_vector_type(2))) float f32x2;
typedef __attribute__((ext_vector_type(4))) unsigned u32x4;
typedef __attribute__((ext_vector_type(2))) unsigned u32x2;
typedef __attribute__((address_space(3))) char lds_char;
#define LDSP(T, p) ((__attribute__((address_space(3))) T*)(p))
#define DEVINL __device__ __forceinline__
#define LAMBDA __attribute__((always_inline))

constexpr int PLANE     = 3072;
constexpr int H_OFF     = 0;
constexpr int R0 = 49152, R1 = 81920, R2 = 114688;
constexpr int STATS_OFF = 147456;
constexpr int LDS_BYTES = STATS_OFF + 6144;
constexpr int HOUT_PLANE = 2048;
__host__ __device__ constexpr int reg_off(int a) { return a == 0 ? R0 : a == 1 ? R1 : R2; }
__host__ __device__ constexpr int blk_off(int s, int di) { return (s & 3) < 3 ? reg_off(s & 3) + di * 8192 : reg_off(di) + 24576; }
__host__ __device__ constexpr int wz2_off(int ks) { return ks < 3 ? R0 + ks * 8192 : R1; }
__host__ __device__ constexpr int hout_off(int ks) { return ks < 3 ? R2 + ks * 8192 : R1 + 8192; }
constexpr unsigned OOB  = 0x40000000u;
constexpr int TENSOR_BYTES = 2 * 128 * 128 * 128 * 2;
constexpr int NTHREADS = 576;

#define STAMP(i) do {} while (0)
#define STORE_AUX 17
#define WAIT_VM(n) asm volatile("s_waitcnt vmcnt(" #n ")" ::: "memory")
#define BAR() asm volatile("s_waitcnt lgkmcnt(0)\n\ts_barrier" ::: "memory")
#define SCHED() __builtin_amdgcn_sched_barrier(0)

template <int N> struct IC { static constexpr int value = N; };
template <int I, int N, class F> DEVINL void static_for(F&& f) { if constexpr (I < N) { f(IC<I>{}); static_for<I + 1, N>(f); } }

DEVINL u32x4 make_rsrc(const void* p, unsigned bytes) {
  unsigned long long a = (unsigned long long)p;
  u32x4 r = {(unsigned)a, (unsigned)(a >> 32) & 0xffffu, bytes, 0x00020000u};
  return r;
}
DEVINL void dma16(u32x4 rsrc, unsigned voff, unsigned soff, unsigned lds_addr) {
  asm volatile("s_mov_b32 m0, %0\n\ts_nop 0\n\tbuffer_load_dwordx4 %1, %2, %3 offen lds" :: "s"(lds_addr), "v"(voff), "s"(rsrc), "s"(soff) : "memory");
}
DEVINL void dma16x4(u32x4 rsrc, unsigned v0, unsigned v1, unsigned v2, unsigned v3, unsigned soff, unsigned lds_addr) {
  asm volatile("s_mov_b32 m0, %0\n\ts_nop 0\n\t"
               "buffer_load_dwordx4 %1, %5, %6 offen lds\n\t"
               "buffer_load_dwordx4 %2, %5, %6 offen offset:1024 lds\n\t"
               "buffer_load_dwordx4 %3, %5, %6 offen offset:2048 lds\n\t"
               "buffer_load_dwordx4 %4, %5, %6 offen offset:3072 lds"
               :: "s"(lds_addr), "v"(v0), "v"(v1), "v"(v2), "v"(v3), "s"(rsrc), "s"(soff) : "memory");
}
DEVINL void dma16x4_nt(u32x4 rsrc, unsigned v0, unsigned v1, unsigned v2, unsigned v3, unsigned soff, unsigned lds_addr) {
  asm volatile("s_mov_b32 m0, %0\n\ts_nop 0\n\t"
               "buffer_load_dwordx4 %1, %5, %6 offen nt lds\n\t"
               "buffer_load_dwordx4 %2, %5, %6 offen offset:1024 nt lds\n\t"
               "buffer_load_dwordx4 %3, %5, %6 offen offset:2048 nt lds\n\t"
               "buffer_load_dwordx4 %4, %5, %6 offen offset:3072 nt lds"
               :: "s"(lds_addr), "v"(v0), "v"(v1), "v"(v2), "v"(v3), "s"(rsrc), "s"(soff) : "memory");
}
DEVINL void dma16_nt(u32x4 rsrc, unsigned voff, unsigned soff, unsigned lds_addr) {
  asm volatile("s_mov_b32 m0, %0\n\ts_nop 0\n\tbuffer_load_dwordx4 %1, %2, %3 offen nt lds" :: "s"(lds_addr), "v"(voff), "s"(rsrc), "s"(soff) : "memory");
}
DEVINL u32x4 ld16_nt(u32x4 rsrc, unsigned voff, unsigned soff) {
  u32x4 r;
  asm volatile("buffer_load_dwordx4 %0, %1, %2, %3 offen nt" : "=v"(r) : "v"(voff), "s"(rsrc), "s"(soff) : "memory");
  return r;
}
DEVINL const lds_char* ldsp(unsigned a) { return (const lds_char*)(size_t)a; }
DEVINL bf16x8 lds_b128(const lds_char* p) { return *LDSP(const bf16x8, p); }
DEVINL bf16x8 lds_tr_frag(const lds_char* p) {
  s16x4 lo = __builtin_amdgcn_ds_read_tr16_b64_v4i16(LDSP(s16x4, p));
  s16x4 hi = __builtin_amdgcn_ds_read_tr16_b64_v4i16(LDSP(s16x4, p + 1024));
  s16x8 r = __builtin_shufflevector(lo, hi, 0, 1, 2, 3, 4, 5, 6, 7);
  return __builtin_bit_cast(bf16x8, r);
}
DEVINL f32x4 mfma(bf16x8 a, bf16x8 b, f32x4 c) { return __builtin_amdgcn_mfma_f32_16x16x32_bf16(a, b, c, 0, 0, 0); }
DEVINL unsigned pk2(float lo, float hi) {
  f32x2 f = {lo, hi};
  return __builtin_bit_cast(unsigned, __builtin_convertvector(f, bf16x2));
}
DEVINL unsigned pk2v(f32x2 f) { return __builtin_bit_cast(unsigned, __builtin_convertvector(f, bf16x2)); }
DEVINL float bf_lo(unsigned u) { return __builtin_bit_cast(float, u << 16); }
DEVINL float bf_hi(unsigned u) { return __builtin_bit_cast(float, u & 0xffff0000u); }
DEVINL unsigned f2u(float f) { return __builtin_bit_cast(unsigned, f); }
DEVINL float u2f(unsigned u) { return __builtin_bit_cast(float, u); }
DEVINL f32x4 unpack4(u32x2 v) { f32x4 r = {bf_lo(v[0]), bf_hi(v[0]), bf_lo(v[1]), bf_hi(v[1])}; return r; }
DEVINL f32x2 lo2(f32x4 v) { return __builtin_shufflevector(v, v, 0, 1); }
DEVINL f32x2 hi2(f32x4 v) { return __builtin_shufflevector(v, v, 2, 3); }
DEVINL f32x2 bc2(float v) { f32x2 r = {v, v}; return r; }
DEVINL f32x2 fma2(f32x2 a, f32x2 b, f32x2 c) { return __builtin_elementwise_fma(a, b, c); }
DEVINL void unpack_chunk(u32x4 L, f32x4& t0, f32x4& t1) {
  auto a = __builtin_amdgcn_permlane16_swap(L[0], L[2], false, false);
  auto b = __builtin_amdgcn_permlane16_swap(L[1], L[3], false, false);
  t0[0] = bf_lo(a[0]); t0[1] = bf_hi(a[0]); t0[2] = bf_lo(b[0]); t0[3] = bf_hi(b[0]);
  t1[0] = bf_lo(a[1]); t1[1] = bf_hi(a[1]); t1[2] = bf_lo(b[1]); t1[3] = bf_hi(b[1]);
}
DEVINL u32x4 pack_chunk(f32x4 t0, f32x4 t1) {
  unsigned p00 = pk2(t0[0], t0[1]), p01 = pk2(t0[2], t0[3]);
  unsigned p10 = pk2(t1[0], t1[1]), p11 = pk2(t1[2], t1[3]);
  auto a = __builtin_amdgcn_permlane16_swap(p00, p10, false, false);
  auto b = __builtin_amdgcn_permlane16_swap(p01, p11, false, false);
  u32x4 r = {a[0], b[0], a[1], b[1]};
  return r;
}
DEVINL float reduce4(float a, float b, float c, float d) {
  auto r0 = __builtin_amdgcn_permlane16_swap(f2u(a), f2u(b), false, false);
  auto r1 = __builtin_amdgcn_permlane16_swap(f2u(c), f2u(d), false, false);
  float ab = u2f(r0[0]) + u2f(r0[1]);
  float cd = u2f(r1[0]) + u2f(r1[1]);
  auto r2 = __builtin_amdgcn_permlane32_swap(f2u(ab), f2u(cd), false, false);
  return u2f(r2[0]) + u2f(r2[1]);
}

__global__ __launch_bounds__(NTHREADS, 3)
void controlnet_fused(const __hip_bfloat16* __restrict__ xf, const __hip_bfloat16* __restrict__ x,
                      const __hip_bfloat16* __restrict__ c, const __hip_bfloat16* __restrict__ wz1,
                      const __hip_bfloat16* __restrict__ wz2, const __hip_bfloat16* __restrict__ wconv,
                      __hip_bfloat16* __restrict__ out
                      ) {
  extern __shared__ __attribute__((aligned(16))) char smem_generic[];
  const unsigned lds0 = (unsigned)(size_t)(lds_char*)smem_generic;

  const int tid = threadIdx.x;
  const int lane = tid & 63;
  const int wv = __builtin_amdgcn_readfirstlane(tid >> 6);
  const int q = lane >> 4, px = lane & 15;
  const int bid = blockIdx.x;
  const int tile = (bid & 7) * 32 + (bid >> 3);
  const int bimg = tile >> 7, ty0 = ((tile >> 3) & 15) * 8, tx0 = (tile & 7) * 16;
  const u32x4 r_c  = make_rsrc(c, TENSOR_BYTES), r_x = make_rsrc(x, TENSOR_BYTES);

  auto grp_row = [&](int T, int j) LAMBDA -> int { return T == 0 ? j : (j == 0 ? 9 : j + 4); };
  auto row_soff = [&](int r) LAMBDA -> unsigned {
    const int gy = ty0 - 1 + r;
    return (unsigned)gy < 128u ? (unsigned)(((bimg * 128 + gy) * 128 + tx0) * 256) : OOB;
  };
  auto row_voff = [&](int w_) LAMBDA -> unsigned { const int p = 4 * w_ + q; return (unsigned)(p * 256 + ((px ^ p) * 16)); };
  auto col_voff = [&](int T, int w_) LAMBDA -> unsigned {
    const int p = 4 * w_ + q;
    const int gy = ty0 - 1 + 5 * T + (p >> 1), gx = tx0 - 1 + ((p & 1) ? 17 : 0);
    const bool ok = p < 10 && (unsigned)gy < 128u && (unsigned)gx < 128u;
    return ok ? (unsigned)(((bimg * 128 + gy) * 128 + gx) * 256 + ((px ^ p) * 16)) : OOB;
  };

  if (wv == 8) {
    unsigned rv[4];
#pragma unroll
    for (int w_ = 0; w_ < 4; ++w_) rv[w_] = row_voff(w_) - w_ * 1024;
    auto rows = [&](int j) LAMBDA {
#pragma unroll
      for (int T = 0; T < 2; ++T) {
        const unsigned so = row_soff(grp_row(T, j));
        const unsigned dst = lds0 + reg_off(j >> 1) + (T * 2 + (j & 1)) * 8192;
        dma16x4_nt(r_c, rv[0], rv[1], rv[2], rv[3], so, dst);
        dma16x4_nt(r_x, rv[0], rv[1], rv[2], rv[3], so, dst + 4096);
      }
    };
    BAR();
    rows(2); rows(3);
    BAR();
    rows(4);
#pragma unroll
    for (int T = 0; T < 2; ++T) {
      unsigned cv[4];
#pragma unroll
      for (int w_ = 0; w_ < 4; ++w_) { unsigned o = col_voff(T, w_); cv[w_] = o == OOB ? OOB : o - w_ * 1024; }
      const unsigned dst = lds0 + R2 + (T * 2 + 1) * 8192;
      dma16x4_nt(r_c, cv[0], cv[1], cv[2], cv[3], 0, dst);
      dma16x4_nt(r_x, cv[0], cv[1], cv[2], cv[3], 0, dst + 4096);
    }
    const u32x4 r_w2 = make_rsrc(wz2, 32768), r_wc = make_rsrc(wconv, 9 * 32768);
    const unsigned vA = (unsigned)(q * 256 + ((px ^ (q << 1)) * 16));
    const unsigned vB = (unsigned)(q * 256 + ((px ^ ((q | 4) << 1)) * 16));
    auto block = [&](u32x4 rs, int src, int dst) LAMBDA {
      dma16x4(rs, vA, vA, vB, vB, src, lds0 + dst);
      dma16x4(rs, vA, vA, vB, vB, src + 4096, lds0 + dst + 4096);
    };
    auto stage = [&](int s) LAMBDA {
#pragma unroll
      for (int di = 0; di < 3; ++di) block(r_wc, (di * 3 + (s >> 2)) * 32768 + (s & 3) * 8192, blk_off(s, di));
    };
    WAIT_VM(32); BAR();
    stage(0);
    WAIT_VM(24); BAR();
    stage(1);
    WAIT_VM(24); BAR();
    stage(2); stage(3);
    WAIT_VM(24); BAR();
    stage(4);
    WAIT_VM(0); BAR();
    stage(5); stage(6);
    WAIT_VM(0); BAR();
    stage(7); stage(8);
    WAIT_VM(0); BAR();
    stage(9); stage(10);
    WAIT_VM(0); BAR();
    stage(11);
    block(r_w2, 0, wz2_off(0)); block(r_w2, 8192, wz2_off(1)); block(r_w2, 16384, wz2_off(2));
    WAIT_VM(0); BAR();
    block(r_w2, 24576, wz2_off(3));
    WAIT_VM(0); BAR();
    return;
  }

  const int T = wv >> 2, w = wv & 3;
  STAMP(0);
  {
    const u32x4 r_w1 = make_rsrc(wz1, 32768);
    const int wk = 4 * wv + q;
    const int wf = (wk & 3) | (((wk >> 3) & 1) << 2);
    const unsigned wvoff = (unsigned)(wk * 256 + ((px ^ (wf << 1)) * 16));
#pragma unroll
    for (int ks = 0; ks < 4; ++ks) dma16(r_w1, wvoff, ks * 8192, lds0 + wv * 1024 + H_OFF + ks * 8192);
    const unsigned rv = row_voff(w);
#pragma unroll
    for (int j = 0; j < 2; ++j) {
      const unsigned so = row_soff(grp_row(T, j));
      const unsigned dst = lds0 + R0 + (T * 2 + j) * 8192 + w * 1024;
      dma16_nt(r_c, rv, so, dst);
      dma16_nt(r_x, rv, so, dst + 4096);
    }
  }
  STAMP(13);
  const u32x4 r_xf = make_rsrc(xf, TENSOR_BYTES);
  __amdgpu_buffer_rsrc_t r_o  = __builtin_amdgcn_make_buffer_rsrc((void*)out, 0, TENSOR_BYTES, 0x00020000);
  const int aq = (lane & 15) >> 2, ap = lane & 3;
  const int af = aq | ((q & 1) << 2);
  const unsigned ab0 = (unsigned)((8 * q + aq) * 256 + (((2 * w) ^ af) * 32) + 8 * ap);
  const unsigned aH0 = lds0 + ab0, aH1 = lds0 + (ab0 ^ 32);
  const unsigned aR0 = aH0 + R0, aR1 = aH1 + R0;
  const unsigned aS0 = aH0 + R2, aS1 = aH1 + R2;
  auto aptr = [&](int off, int t) LAMBDA -> const lds_char* {
    if (off < R0) return ldsp(t ? aH1 : aH0) + off;
    if (off < R2) return ldsp(t ? aR1 : aR0) + (off - R0);
    return ldsp(t ? aS1 : aS0) + (off - R2);
  };
  unsigned hw[6];
  {
    const unsigned hwbase = lds0 + H_OFF + (4 * w + (q >> 1)) * PLANE + (q & 1) * 8;
#pragma unroll
    for (int j = 0; j < 5; ++j) hw[j] = hwbase + (grp_row(T, j) * 18 + px + 1) * 16;
    hw[5] = hwbase + (px < 10 ? (5 * T + (px >> 1)) * 18 + ((px & 1) ? 17 : 0) : 180 + 6 * T + (px - 10)) * 16;
  }
  unsigned cb[4], cb2[4];
#pragma unroll
  for (int ks = 0; ks < 4; ++ks) { cb[ks] = lds0 + R0 + T * 16384 + px * 256 + (((4 * ks + q) ^ px) * 16); cb2[ks] = cb[ks] + 65536; }
  const unsigned xb0 = lds0 + R0 + T * 16384 + 4096 + px * 256 + (((4 * w + (q >> 1)) ^ px) * 16) + (q & 1) * 8;
  const unsigned xb1 = lds0 + R0 + T * 16384 + 4096 + px * 256 + (((4 * w + 2 + (q >> 1)) ^ px) * 16) + (q & 1) * 8;
  const unsigned stw = lds0 + STATS_OFF + (w >> 1) * 3072 + ((T * 6 + (q >> 1)) * 16 + px) * 16 + (w & 1) * 8 + (q & 1) * 4;
  const unsigned str = lds0 + STATS_OFF + (T * 6 * 16 + px) * 16;

  f32x4 hin[6][2];
  bf16x8 A1f[4][2];
  bf16x8 Af[2][3][2], Bf[2][6];
  u32x4 xfr[4];
  bf16x8 Cb[2][4]; u32x2 Xb[2][2];
  auto z1_read = [&](auto kc) LAMBDA {
    constexpr int k = decltype(kc)::value;
#pragma unroll
    for (int jj = 0; jj < 2; ++jj) {
      if constexpr (k < 2) {
        Xb[jj][0] = *LDSP(const u32x2, ldsp(xb0) + k * 32768 + jj * 8192);
        Xb[jj][1] = *LDSP(const u32x2, ldsp(xb1) + k * 32768 + jj * 8192);
      } else {
        Xb[jj][0] = *LDSP(const u32x2, ldsp(xb0 + 65536) + jj * 8192);
        Xb[jj][1] = *LDSP(const u32x2, ldsp(xb1 + 65536) + jj * 8192);
      }
    }
#pragma unroll
    for (int ks = 0; ks < 4; ++ks)
#pragma unroll
      for (int jj = 0; jj < 2; ++jj)
        Cb[jj][ks] = k < 2 ? lds_b128(ldsp(cb[ks]) + k * 32768 + jj * 8192) : lds_b128(ldsp(cb2[ks]) + jj * 8192);
  };
  auto z1_unpack = [&](auto kc) LAMBDA {
    constexpr int k = decltype(kc)::value;
#pragma unroll
    for (int jj = 0; jj < 2; ++jj) {
      hin[2 * k + jj][0] = unpack4(Xb[jj][0]);
      hin[2 * k + jj][1] = unpack4(Xb[jj][1]);
    }
  };
  auto z1_mma1 = [&](auto kc, auto ic) LAMBDA {
    constexpr int k = decltype(kc)::value, i = decltype(ic)::value;
    constexpr int ks = i >> 2, jj = (i >> 1) & 1, t = i & 1;
    hin[2 * k + jj][t] = mfma(A1f[ks][t], Cb[jj][ks], hin[2 * k + jj][t]);
  };
  auto z1_stats = [&](auto kc) LAMBDA {
    constexpr int k = decltype(kc)::value;
    float st[4];
#pragma unroll
    for (int jj = 0; jj < 2; ++jj) {
      const int j = 2 * k + jj;
      f32x2 a = (lo2(hin[j][0]) + hi2(hin[j][0])) + (lo2(hin[j][1]) + hi2(hin[j][1]));
      f32x2 b = lo2(hin[j][0]) * lo2(hin[j][0]);
      b = fma2(hi2(hin[j][0]), hi2(hin[j][0]), b);
      b = fma2(lo2(hin[j][1]), lo2(hin[j][1]), b);
      b = fma2(hi2(hin[j][1]), hi2(hin[j][1]), b);
      st[2 * jj] = a[0] + a[1];
      st[2 * jj + 1] = b[0] + b[1];
    }
    float r = reduce4(st[0], st[1], st[2], st[3]);
    *LDSP(float, (lds_char*)(size_t)stw + 2 * k * 256) = r;
  };
  f32x4 sp0, sp1;
  const unsigned strh = str + (q >> 1) * 256;
  auto si_read = [&](auto kc) LAMBDA {
    constexpr int k = decltype(kc)::value;
    sp0 = *LDSP(const f32x4, ldsp(strh) + 2 * k * 256);
    sp1 = *LDSP(const f32x4, ldsp(strh) + 2 * k * 256 + 3072);
  };
  auto si_math = [&](auto kc, auto hook) LAMBDA {
    constexpr int k = decltype(kc)::value;
    float rs2[2], nb2[2];
    {
      f32x2 m = ((lo2(sp0) + hi2(sp0)) + (lo2(sp1) + hi2(sp1))) * bc2(1.f / 128.f);
      float var = __builtin_fmaxf(__builtin_fmaf(-m[0], m[0], m[1]), 0.f);
      float r = __builtin_amdgcn_rsqf(var + 1e-5f) * -1.4426950408889634f;
      float n = -m[0] * r;
      auto a = __builtin_amdgcn_permlane32_swap(f2u(r), f2u(r), false, false);
      auto b = __builtin_amdgcn_permlane32_swap(f2u(n), f2u(n), false, false);
      rs2[0] = u2f(a[0]); rs2[1] = u2f(a[1]); nb2[0] = u2f(b[0]); nb2[1] = u2f(b[1]);
    }
    f32x2 t[8], d[8];
#pragma unroll
    for (int g = 0; g < 4; ++g) {
      t[2 * g]     = fma2(lo2(hin[2 * k + (g >> 1)][g & 1]), bc2(rs2[g >> 1]), bc2(nb2[g >> 1]));
      t[2 * g + 1] = fma2(hi2(hin[2 * k + (g >> 1)][g & 1]), bc2(rs2[g >> 1]), bc2(nb2[g >> 1]));
    }
    SCHED();
    static_for<0, 16>([&](auto ic) LAMBDA {
      constexpr int i = decltype(ic)::value;
      hook(ic);
      d[i >> 1][i & 1] = __builtin_amdgcn_exp2f(t[i >> 1][i & 1]);
      SCHED();
    });
#pragma unroll
    for (int p = 0; p < 8; ++p) d[p] = d[p] + bc2(1.f);
    f32x2 P[4]; float rr[4];
#pragma unroll
    for (int g = 0; g < 4; ++g) P[g] = d[2 * g] * d[2 * g + 1];
#pragma unroll
    for (int g = 0; g < 4; ++g) rr[g] = __builtin_amdgcn_rcpf(P[g][0] * P[g][1]);
#pragma unroll
    for (int g = 0; g < 4; ++g) { f32x2 a = t[2 * g] * d[2 * g + 1]; t[2 * g + 1] = t[2 * g + 1] * d[2 * g]; t[2 * g] = a; }
#pragma unroll
    for (int g = 0; g < 4; ++g) {
      f32x2 sw = __builtin_shufflevector(P[g], P[g], 1, 0);
      P[g] = sw * bc2(rr[g] * -0.6931471805599453f);
    }
#pragma unroll
    for (int g = 0; g < 4; ++g) {
      u32x2 pk = {pk2v(t[2 * g] * P[g]), pk2v(t[2 * g + 1] * P[g])};
      *LDSP(u32x2, (lds_char*)(size_t)hw[2 * k + (g >> 1)] + 2 * (g & 1) * PLANE) = pk;
    }
  };

  WAIT_VM(4);
  BAR();
#pragma unroll
  for (int ks = 0; ks < 4; ++ks) {
    A1f[ks][0] = lds_tr_frag(aptr(H_OFF + ks * 8192, 0));
    A1f[ks][1] = lds_tr_frag(aptr(H_OFF + ks * 8192, 1));
  }
  WAIT_VM(0);
  BAR();
  STAMP(1);
  z1_read(IC<0>{});
  SCHED();
  z1_unpack(IC<0>{});
  static_for<0, 16>([&](auto ic) LAMBDA { z1_mma1(IC<0>{}, ic); });
  z1_stats(IC<0>{});
  SCHED();
  STAMP(2);
  BAR();
  STAMP(3);
  si_read(IC<0>{});
  z1_read(IC<1>{});
  SCHED();
  z1_unpack(IC<1>{});
  si_math(IC<0>{}, [&](auto ic) LAMBDA {
 z1_mma1(IC<1>{}, ic);
 });
  z1_stats(IC<1>{});
  SCHED();
  STAMP(4);
  BAR();
  STAMP(5);
  si_read(IC<1>{});
  z1_read(IC<2>{});
  const unsigned ooff = (unsigned)(px * 256 + (4 * w + (((q & 1) << 1) | (q >> 1))) * 16);
  const unsigned osoff = (unsigned)(((bimg * 128 + ty0 + 4 * T) * 128 + tx0) * 256);
#pragma unroll
  for (int i = 0; i < 4; ++i) xfr[i] = ld16_nt(r_xf, ooff, osoff + i * 32768);
  SCHED();
  z1_unpack(IC<2>{});
  si_math(IC<1>{}, [&](auto ic) LAMBDA {
 z1_mma1(IC<2>{}, ic);
 });
  z1_stats(IC<2>{});
  SCHED();
  STAMP(6);
  BAR();
  STAMP(7);
  si_read(IC<2>{});
#pragma unroll
  for (int di = 0; di < 3; ++di) {
    Af[0][di][0] = lds_tr_frag(aptr(blk_off(0, di), 0));
    Af[0][di][1] = lds_tr_frag(aptr(blk_off(0, di), 1));
  }
  SCHED();
  si_math(IC<2>{}, [&](auto ic) LAMBDA {});
  f32x4 acc[4][2];
#pragma unroll
  for (int i = 0; i < 4; ++i) { acc[i][0] = hin[i + 1][0]; acc[i][1] = hin[i + 1][1]; }
  SCHED();
  STAMP(8);
  BAR();
  STAMP(9);

  const lds_char* hb = ldsp(lds0 + H_OFF + q * PLANE + (T * 4 * 18 + px) * 16);
#pragma unroll
  for (int rr = 0; rr < 6; ++rr) Bf[0][rr] = lds_b128(hb + rr * 18 * 16);
  bf16x8 A2f[4][2];
  f32x4 o2[4][2];
  const unsigned owbase = lds0 + (w < 3 ? R2 + w * 8192 : R1 + 8192) + (q >> 1) * HOUT_PLANE + (q & 1) * 8 + (4 * T * 16 + px) * 16;
  static_for<0, 12>([&](auto sc) LAMBDA {
    constexpr int s = decltype(sc)::value;
    constexpr int cur = s & 1, nxt = cur ^ 1;
    SCHED();
    if constexpr (s > 0 && (s & 1) == 0) BAR();
    if constexpr (s == 10) {
      asm volatile("s_waitcnt vmcnt(0)" : "+v"(xfr[0]), "+v"(xfr[1]), "+v"(xfr[2]), "+v"(xfr[3]) :: "memory");
    }
    SCHED();
    if constexpr (s + 1 < 12) {
      constexpr int s1 = s + 1, dj = s1 >> 2, ks = s1 & 3;
#pragma unroll
      for (int di = 0; di < 3; ++di) {
        Af[nxt][di][0] = lds_tr_frag(aptr(blk_off(s1, di), 0));
        Af[nxt][di][1] = lds_tr_frag(aptr(blk_off(s1, di), 1));
      }
#pragma unroll
      for (int rr = 0; rr < 6; ++rr) Bf[nxt][rr] = lds_b128(hb + ks * 4 * PLANE + (rr * 18 + dj) * 16);
    } else {
#pragma unroll
      for (int ks = 0; ks < 3; ++ks) {
        A2f[ks][0] = lds_tr_frag(aptr(wz2_off(ks), 0));
        A2f[ks][1] = lds_tr_frag(aptr(wz2_off(ks), 1));
      }
    }
    if constexpr (s == 10) {
#pragma unroll
      for (int i = 0; i < 4; ++i) unpack_chunk(xfr[i], o2[i][0], o2[i][1]);
    }
    if constexpr (s < 11) {
#pragma unroll
      for (int di = 0; di < 3; ++di)
#pragma unroll
        for (int i = 0; i < 4; ++i) {
          acc[i][0] = mfma(Af[cur][di][0], Bf[cur][i + di], acc[i][0]);
          acc[i][1] = mfma(Af[cur][di][1], Bf[cur][i + di], acc[i][1]);
        }
    } else {
#pragma unroll
      for (int i = 0; i < 4; ++i)
#pragma unroll
        for (int di = 0; di < 3; ++di) {
          acc[i][0] = mfma(Af[cur][di][0], Bf[cur][i + di], acc[i][0]);
          acc[i][1] = mfma(Af[cur][di][1], Bf[cur][i + di], acc[i][1]);
        }
#pragma unroll
      for (int i = 0; i < 4; ++i)
#pragma unroll
        for (int t = 0; t < 2; ++t) {
          u32x2 pk = {pk2(acc[i][t][0], acc[i][t][1]), pk2(acc[i][t][2], acc[i][t][3])};
          *LDSP(u32x2, (lds_char*)(size_t)owbase + 2 * t * HOUT_PLANE + i * 256) = pk;
        }
    }
    if constexpr (s == 10) {
#pragma unroll
      for (int g = 0; g < 18; ++g) {
        __builtin_amdgcn_sched_group_barrier(0x008, 1, 0);
        __builtin_amdgcn_sched_group_barrier(0x100, 1, 0);
        __builtin_amdgcn_sched_group_barrier(0x002, 2, 0);
      }
      __builtin_amdgcn_sched_group_barrier(0x008, 6, 0);
    } else if constexpr (s + 1 < 12) {
#pragma unroll
      for (int g = 0; g < 18; ++g) {
        __builtin_amdgcn_sched_group_barrier(0x008, 1, 0);
        __builtin_amdgcn_sched_group_barrier(0x100, 1, 0);
      }
      __builtin_amdgcn_sched_group_barrier(0x008, 6, 0);
    } else {
#pragma unroll
      for (int g = 0; g < 12; ++g) {
        __builtin_amdgcn_sched_group_barrier(0x008, 1, 0);
        __builtin_amdgcn_sched_group_barrier(0x100, 1, 0);
      }
#pragma unroll
      for (int g = 0; g < 6; ++g) {
        __builtin_amdgcn_sched_group_barrier(0x008, 2, 0);
        __builtin_amdgcn_sched_group_barrier(0x002, 4, 0);
        __builtin_amdgcn_sched_group_barrier(0x200, 2, 0);
      }
    }
  });
  SCHED();
  STAMP(10);
  BAR();
  STAMP(11);
  {
    bf16x8 B[4][4];
    const unsigned hr = lds0 + q * HOUT_PLANE + (4 * T * 16 + px) * 16;
    const unsigned hr2 = hr + R2, hr1 = hr + R1 + 8192;
#pragma unroll
    for (int ks = 0; ks < 4; ++ks) B[ks][0] = lds_b128(ks < 3 ? ldsp(hr2) + ks * 8192 : ldsp(hr1));
    A2f[3][0] = lds_tr_frag(aptr(wz2_off(3), 0));
    A2f[3][1] = lds_tr_frag(aptr(wz2_off(3), 1));
#pragma unroll
    for (int i = 1; i < 4; ++i)
#pragma unroll
      for (int ks = 0; ks < 4; ++ks) B[ks][i] = lds_b128((ks < 3 ? ldsp(hr2) + ks * 8192 : ldsp(hr1)) + i * 256);
    SCHED();
    static_for<0, 4>([&](auto ic) LAMBDA {
      constexpr int i = decltype(ic)::value;
#pragma unroll
      for (int ks = 0; ks < 4; ++ks) {
        o2[i][0] = mfma(A2f[ks][0], B[ks][i], o2[i][0]);
        o2[i][1] = mfma(A2f[ks][1], B[ks][i], o2[i][1]);
      }
      if constexpr (i > 0) {
        u32x4 pk = pack_chunk(o2[i - 1][0], o2[i - 1][1]);
        __builtin_amdgcn_raw_buffer_store_b128(pk, r_o, ooff, osoff + (i - 1) * 32768, STORE_AUX);
#pragma unroll
        for (int g = 0; g < 6; ++g) {
          __builtin_amdgcn_sched_group_barrier(0x008, 1, 0);
          __builtin_amdgcn_sched_group_barrier(0x002, 1, 0);
        }
        __builtin_amdgcn_sched_group_barrier(0x008, 2, 0);
      }
      SCHED();
    });
    u32x4 pk = pack_chunk(o2[3][0], o2[3][1]);
    __builtin_amdgcn_raw_buffer_store_b128(pk, r_o, ooff, osoff + 3 * 32768, STORE_AUX);
  }
  STAMP(12);
}

extern "C" void kernel_launch(void* const* d_in, const int* in_sizes, int n_in,
                              void* d_out, int out_size, void* d_ws, size_t ws_size,
                              hipStream_t stream) {
  const __hip_bfloat16* xf  = (const __hip_bfloat16*)d_in[0];
  const __hip_bfloat16* x   = (const __hip_bfloat16*)d_in[1];
  const __hip_bfloat16* c   = (const __hip_bfloat16*)d_in[2];
  const __hip_bfloat16* wz1 = (const __hip_bfloat16*)d_in[3];
  const __hip_bfloat16* wz2 = (const __hip_bfloat16*)d_in[4];
  const __hip_bfloat16* wc  = (const __hip_bfloat16*)d_in[5];
  (void)hipFuncSetAttribute((const void*)controlnet_fused, hipFuncAttributeMaxDynamicSharedMemorySize, LDS_BYTES);
  controlnet_fused<<<256, NTHREADS, LDS_BYTES, stream>>>(xf, x, c, wz1, wz2, wc, (__hip_bfloat16*)d_out
  );
}
```

```cpp
#define XF_NT
#include <hip/hip_runtime.h>
#include <hip/hip_bf16.h>

typedef __attribute__((ext_vector_type(8))) __bf16 bf16x8;
typedef __attribute__((ext_vector_type(2))) __bf16 bf16x2;
typedef __attribute__((ext_vector_type(4))) short s16x4;
typedef __attribute__((ext_vector_type(8))) short s16x8;
typedef __attribute__((ext_vector_type(4))) float f32x4;
typedef __attribute__((ext_vector_type(2))) float f32x2;
typedef __attribute__((ext_vector_type(4))) unsigned u32x4;
typedef __attribute__((ext_vector_type(2))) unsigned u32x2;
typedef __attribute__((address_space(3))) char lds_char;
#define LDSP(T, p) ((__attribute__((address_space(3))) T*)(p))
#define DEVINL __device__ __forceinline__
#define LAMBDA __attribute__((always_inline))

constexpr int PLANE     = 3072;
constexpr int H_OFF     = 0;
constexpr int R0 = 49152, R1 = 81920, R2 = 114688;
constexpr int STATS_OFF = 147456;
constexpr int XF_OFF    = STATS_OFF;
constexpr int LDS_BYTES = STATS_OFF + 16384;
constexpr int HOUT_PLANE = 2048;
__host__ __device__ constexpr int reg_off(int a) { return a == 0 ? R0 : a == 1 ? R1 : R2; }
__host__ __device__ constexpr int blk_off(int s, int di) { return (s & 3) < 3 ? reg_off(s & 3) + di * 8192 : reg_off(di) + 24576; }
__host__ __device__ constexpr int wz2_off(int ks) { return ks < 3 ? R0 + ks * 8192 : R1; }
__host__ __device__ constexpr int hout_off(int ks) { return ks < 3 ? R2 + ks * 8192 : R1 + 8192; }
constexpr unsigned OOB  = 0x40000000u;
constexpr int TENSOR_BYTES = 2 * 128 * 128 * 128 * 2;
constexpr int NTHREADS = 576;

#define STAMP(i) do {} while (0)
#define STORE_AUX 17
#define WAIT_VM(n) asm volatile("s_waitcnt vmcnt(" #n ")" ::: "memory")
#define BAR() asm volatile("s_waitcnt lgkmcnt(0)\n\ts_barrier" ::: "memory")
#define SCHED() __builtin_amdgcn_sched_barrier(0)

template <int N> struct IC { static constexpr int value = N; };
template <int I, int N, class F> DEVINL void static_for(F&& f) { if constexpr (I < N) { f(IC<I>{}); static_for<I + 1, N>(f); } }

DEVINL u32x4 make_rsrc(const void* p, unsigned bytes) {
  unsigned long long a = (unsigned long long)p;
  u32x4 r = {(unsigned)a, (unsigned)(a >> 32) & 0xffffu, bytes, 0x00020000u};
  return r;
}
DEVINL void dma16(u32x4 rsrc, unsigned voff, unsigned soff, unsigned lds_addr) {
  asm volatile("s_mov_b32 m0, %0\n\ts_nop 0\n\tbuffer_load_dwordx4 %1, %2, %3 offen lds" :: "s"(lds_addr), "v"(voff), "s"(rsrc), "s"(soff) : "memory");
}
DEVINL void dma16_nt(u32x4 rsrc, unsigned voff, unsigned soff, unsigned lds_addr) {
  asm volatile("s_mov_b32 m0, %0\n\ts_nop 0\n\tbuffer_load_dwordx4 %1, %2, %3 offen nt lds" :: "s"(lds_addr), "v"(voff), "s"(rsrc), "s"(soff) : "memory");
}
DEVINL void dma16x4(u32x4 rsrc, unsigned v0, unsigned v1, unsigned v2, unsigned v3, unsigned soff, unsigned lds_addr) {
  asm volatile("s_mov_b32 m0, %0\n\ts_nop 0\n\t"
               "buffer_load_dwordx4 %1, %5, %6 offen lds\n\t"
               "buffer_load_dwordx4 %2, %5, %6 offen offset:1024 lds\n\t"
               "buffer_load_dwordx4 %3, %5, %6 offen offset:2048 lds\n\t"
               "buffer_load_dwordx4 %4, %5, %6 offen offset:3072 lds"
               :: "s"(lds_addr), "v"(v0), "v"(v1), "v"(v2), "v"(v3), "s"(rsrc), "s"(soff) : "memory");
}
DEVINL void dma16x4_nt(u32x4 rsrc, unsigned v0, unsigned v1, unsigned v2, unsigned v3, unsigned soff, unsigned lds_addr) {
  asm volatile("s_mov_b32 m0, %0\n\ts_nop 0\n\t"
               "buffer_load_dwordx4 %1, %5, %6 offen nt lds\n\t"
               "buffer_load_dwordx4 %2, %5, %6 offen offset:1024 nt lds\n\t"
               "buffer_load_dwordx4 %3, %5, %6 offen offset:2048 nt lds\n\t"
               "buffer_load_dwordx4 %4, %5, %6 offen offset:3072 nt lds"
               :: "s"(lds_addr), "v"(v0), "v"(v1), "v"(v2), "v"(v3), "s"(rsrc), "s"(soff) : "memory");
}
DEVINL const lds_char* ldsp(unsigned a) { return (const lds_char*)(size_t)a; }
DEVINL lds_char* ldsw(unsigned a) { return (lds_char*)(size_t)a; }
DEVINL bf16x8 lds_b128(const lds_char* p) { return *LDSP(const bf16x8, p); }
DEVINL s16x4 lds_tr(const lds_char* p) { return __builtin_amdgcn_ds_read_tr16_b64_v4i16(LDSP(s16x4, p)); }
DEVINL bf16x8 join(s16x4 lo, s16x4 hi) { return __builtin_bit_cast(bf16x8, __builtin_shufflevector(lo, hi, 0, 1, 2, 3, 4, 5, 6, 7)); }
DEVINL bf16x8 lds_tr_frag(const lds_char* p) { return join(lds_tr(p), lds_tr(p + 1024)); }
DEVINL f32x4 mfma(bf16x8 a, bf16x8 b, f32x4 c) { return __builtin_amdgcn_mfma_f32_16x16x32_bf16(a, b, c, 0, 0, 0); }
DEVINL unsigned pk2v(f32x2 f) { return __builtin_bit_cast(unsigned, __builtin_convertvector(f, bf16x2)); }
DEVINL f32x2 lo2(f32x4 v) { return __builtin_shufflevector(v, v, 0, 1); }
DEVINL f32x2 hi2(f32x4 v) { return __builtin_shufflevector(v, v, 2, 3); }
DEVINL u32x2 pack4(f32x4 v) { u32x2 r = {pk2v(lo2(v)), pk2v(hi2(v))}; return r; }
DEVINL float bf_lo(unsigned u) { return __builtin_bit_cast(float, u << 16); }
DEVINL float bf_hi(unsigned u) { return __builtin_bit_cast(float, u & 0xffff0000u); }
DEVINL unsigned f2u(float f) { return __builtin_bit_cast(unsigned, f); }
DEVINL float u2f(unsigned u) { return __builtin_bit_cast(float, u); }
DEVINL f32x4 unpack4(u32x2 v) { f32x4 r = {bf_lo(v[0]), bf_hi(v[0]), bf_lo(v[1]), bf_hi(v[1])}; return r; }
DEVINL f32x2 bc2(float v) { f32x2 r = {v, v}; return r; }
DEVINL f32x2 fma2(f32x2 a, f32x2 b, f32x2 c) { return __builtin_elementwise_fma(a, b, c); }
DEVINL float reduce4(float a, float b, float c, float d) {
  auto r0 = __builtin_amdgcn_permlane16_swap(f2u(a), f2u(b), false, false);
  auto r1 = __builtin_amdgcn_permlane16_swap(f2u(c), f2u(d), false, false);
  float ab = u2f(r0[0]) + u2f(r0[1]);
  float cd = u2f(r1[0]) + u2f(r1[1]);
  auto r2 = __builtin_amdgcn_permlane32_swap(f2u(ab), f2u(cd), false, false);
  return u2f(r2[0]) + u2f(r2[1]);
}

__global__ __launch_bounds__(NTHREADS, 3)
void controlnet_fused(const __hip_bfloat16* __restrict__ xf, const __hip_bfloat16* __restrict__ x,
                      const __hip_bfloat16* __restrict__ c, const __hip_bfloat16* __restrict__ wz1,
                      const __hip_bfloat16* __restrict__ wz2, const __hip_bfloat16* __restrict__ wconv,
                      __hip_bfloat16* __restrict__ out
                      ) {
  extern __shared__ __attribute__((aligned(16))) char smem_generic[];
  const unsigned lds0 = (unsigned)(size_t)(lds_char*)smem_generic;

  const int tid = threadIdx.x;
  const int lane = tid & 63;
  const int wv = __builtin_amdgcn_readfirstlane(tid >> 6);
  const int q = lane >> 4, px = lane & 15;
  const int bid = blockIdx.x;
  const int tile = (bid & 7) * 32 + (bid >> 3);
  const int bimg = tile >> 7, ty0 = ((tile >> 3) & 15) * 8, tx0 = (tile & 7) * 16;
  const u32x4 r_c  = make_rsrc(c, TENSOR_BYTES), r_x = make_rsrc(x, TENSOR_BYTES), r_xf = make_rsrc(xf, TENSOR_BYTES);

  auto grp_row = [&](int T, int j) LAMBDA -> int { return T == 0 ? j : (j == 0 ? 9 : j + 4); };
  auto row_soff = [&](int r) LAMBDA -> unsigned {
    const int gy = ty0 - 1 + r;
    return (unsigned)gy < 128u ? (unsigned)(((bimg * 128 + gy) * 128 + tx0) * 256) : OOB;
  };
  auto row_voff = [&](int w_) LAMBDA -> unsigned { const int p = 4 * w_ + q; return (unsigned)(p * 256 + ((px ^ p) * 16)); };
  auto col_voff = [&](int T, int w_) LAMBDA -> unsigned {
    const int p = 4 * w_ + q;
    const int gy = ty0 - 1 + 5 * T + (p >> 1), gx = tx0 - 1 + ((p & 1) ? 17 : 0);
    const bool ok = p < 10 && (unsigned)gy < 128u && (unsigned)gx < 128u;
    return ok ? (unsigned)(((bimg * 128 + gy) * 128 + gx) * 256 + ((px ^ p) * 16)) : OOB;
  };

  if (wv == 8) {
    unsigned rv[4];
#pragma unroll
    for (int w_ = 0; w_ < 4; ++w_) rv[w_] = row_voff(w_) - w_ * 1024;
    auto rows = [&](int j) LAMBDA {
#pragma unroll
      for (int T = 0; T < 2; ++T) {
        const unsigned so = row_soff(grp_row(T, j));
        const unsigned dst = lds0 + reg_off(j >> 1) + (T * 2 + (j & 1)) * 8192;
        dma16x4_nt(r_c, rv[0], rv[1], rv[2], rv[3], so, dst);
        dma16x4_nt(r_x, rv[0], rv[1], rv[2], rv[3], so, dst + 4096);
      }
    };
    unsigned tw[4];
    {
      const u32x4 r_w2t = make_rsrc(wz2, 32768), r_wct = make_rsrc(wconv, 9 * 32768);
      const unsigned sl = (unsigned)(bid >> 3);
      const unsigned o0 = sl * 9216 + lane * 128;
      const unsigned o1 = lane < 8 ? sl * 9216 + (64 + lane) * 128 : OOB;
      const unsigned o2 = lane < 8 ? sl * 1024 + lane * 128 : OOB;
      asm volatile("buffer_load_dword %0, %1, %2, 0 offen" : "=v"(tw[0]) : "v"(o0), "s"(r_wct) : "memory");
      asm volatile("buffer_load_dword %0, %1, %2, 0 offen" : "=v"(tw[1]) : "v"(o1), "s"(r_wct) : "memory");
      asm volatile("buffer_load_dword %0, %1, %2, 0 offen" : "=v"(tw[2]) : "v"(o2), "s"(r_w2t) : "memory");
      tw[3] = 0;
    }
    BAR();
    rows(2); rows(3);
    BAR();
    rows(4);
#pragma unroll
    for (int T = 0; T < 2; ++T) {
      unsigned cv[4];
#pragma unroll
      for (int w_ = 0; w_ < 4; ++w_) { unsigned o = col_voff(T, w_); cv[w_] = o == OOB ? OOB : o - w_ * 1024; }
      const unsigned dst = lds0 + R2 + (T * 2 + 1) * 8192;
      dma16x4_nt(r_c, cv[0], cv[1], cv[2], cv[3], 0, dst);
      dma16x4_nt(r_x, cv[0], cv[1], cv[2], cv[3], 0, dst + 4096);
    }
    const u32x4 r_w2 = make_rsrc(wz2, 32768), r_wc = make_rsrc(wconv, 9 * 32768);
    const unsigned vA = (unsigned)(q * 256 + ((px ^ (q << 1)) * 16));
    const unsigned vB = (unsigned)(q * 256 + ((px ^ ((q | 4) << 1)) * 16));
    auto block = [&](u32x4 rs, int src, int dst) LAMBDA {
      dma16x4(rs, vA, vA, vB, vB, src, lds0 + dst);
      dma16x4(rs, vA, vA, vB, vB, src + 4096, lds0 + dst + 4096);
    };
    auto stage = [&](int s) LAMBDA {
#pragma unroll
      for (int di = 0; di < 3; ++di) block(r_wc, (di * 3 + (s >> 2)) * 32768 + (s & 3) * 8192, blk_off(s, di));
    };
    WAIT_VM(32); BAR();
    stage(0);
    WAIT_VM(24); BAR();
    stage(1);
    WAIT_VM(24); BAR();
    stage(2); stage(3);
    WAIT_VM(48); BAR();
    stage(4);
    WAIT_VM(24); BAR();
    stage(5);
    WAIT_VM(0); BAR();
    stage(6); stage(7);
    WAIT_VM(0); BAR();
    stage(8); stage(9);
    WAIT_VM(0); BAR();
    stage(10); stage(11);
    WAIT_VM(0); BAR();
    block(r_w2, 0, wz2_off(0)); block(r_w2, 8192, wz2_off(1)); block(r_w2, 16384, wz2_off(2)); block(r_w2, 24576, wz2_off(3));
    WAIT_VM(0); BAR();
    asm volatile("" :: "v"(tw[0]), "v"(tw[1]), "v"(tw[2]), "v"(tw[3]));
    return;
  }

  const int T = wv >> 2, w = wv & 3;
  STAMP(0);
  {
    const u32x4 r_w1 = make_rsrc(wz1, 32768);
    const int wk = 4 * wv + q;
    const int wf = (wk & 3) | (((wk >> 3) & 1) << 2);
    const unsigned wvoff = (unsigned)(wk * 256 + ((px ^ (wf << 1)) * 16));
#pragma unroll
    for (int ks = 0; ks < 4; ++ks) dma16(r_w1, wvoff, ks * 8192, lds0 + wv * 1024 + H_OFF + ks * 8192);
    const unsigned rv = row_voff(w);
#pragma unroll
    for (int j = 0; j < 2; ++j) {
      const unsigned so = row_soff(grp_row(T, j));
      const unsigned dst = lds0 + R0 + (T * 2 + j) * 8192 + w * 1024;
      dma16_nt(r_c, rv, so, dst);
      dma16_nt(r_x, rv, so, dst + 4096);
    }
  }
  __amdgpu_buffer_rsrc_t r_o = __builtin_amdgcn_make_buffer_rsrc((void*)out, 0, TENSOR_BYTES, 0x00020000);
  const int aq = (lane & 15) >> 2, ap = lane & 3;
  const int af = aq | ((q & 1) << 2);
  const unsigned ab0 = (unsigned)((8 * q + aq) * 256 + (((2 * w) ^ af) * 32) + 8 * ap);
  const unsigned aH0 = lds0 + ab0, aH1 = lds0 + (ab0 ^ 32);
  const unsigned aR0 = aH0 + R0, aR1 = aH1 + R0;
  const unsigned aS0 = aH0 + R2, aS1 = aH1 + R2;
  auto aptr = [&](int off, int t) LAMBDA -> const lds_char* {
    if (off < R0) return ldsp(t ? aH1 : aH0) + off;
    if (off < R2) return ldsp(t ? aR1 : aR0) + (off - R0);
    return ldsp(t ? aS1 : aS0) + (off - R2);
  };
  unsigned hw[6];
  {
    const unsigned hwbase = lds0 + H_OFF + (4 * w + (q >> 1)) * PLANE + (q & 1) * 8;
#pragma unroll
    for (int j = 0; j < 5; ++j) hw[j] = hwbase + (grp_row(T, j) * 18 + px + 1) * 16;
    hw[5] = hwbase + (px < 10 ? (5 * T + (px >> 1)) * 18 + ((px & 1) ? 17 : 0) : 180 + 6 * T + (px - 10)) * 16;
  }
  unsigned cb[4], cb2[4];
#pragma unroll
  for (int ks = 0; ks < 4; ++ks) { cb[ks] = lds0 + R0 + T * 16384 + px * 256 + (((4 * ks + q) ^ px) * 16); cb2[ks] = cb[ks] + 65536; }
  const unsigned xb0 = lds0 + R0 + T * 16384 + 4096 + px * 256 + (((4 * w + (q >> 1)) ^ px) * 16) + (q & 1) * 8;
  const unsigned xb1 = lds0 + R0 + T * 16384 + 4096 + px * 256 + (((4 * w + 2 + (q >> 1)) ^ px) * 16) + (q & 1) * 8;
  const unsigned xb2 = xb0 + 65536, xb3 = xb1 + 65536;
  const unsigned stw = lds0 + STATS_OFF + (w >> 1) * 3072 + ((T * 6 + (q >> 1)) * 16 + px) * 16 + (w & 1) * 8 + (q & 1) * 4;
  const unsigned strh = lds0 + STATS_OFF + ((T * 6 + (q >> 1)) * 16 + px) * 16;

  f32x4 hin[6][2];
  bf16x8 A1f[4][2];
  bf16x8 Cb[2][4]; u32x2 Xb[2][2];
  auto z1_read = [&](auto kc) LAMBDA {
    constexpr int k = decltype(kc)::value;
#pragma unroll
    for (int jj = 0; jj < 2; ++jj) {
      Xb[jj][0] = *LDSP(const u32x2, ldsp(k < 2 ? xb0 : xb2) + (k & 1) * 32768 + jj * 8192);
      Xb[jj][1] = *LDSP(const u32x2, ldsp(k < 2 ? xb1 : xb3) + (k & 1) * 32768 + jj * 8192);
    }
#pragma unroll
    for (int ks = 0; ks < 4; ++ks)
#pragma unroll
      for (int jj = 0; jj < 2; ++jj) Cb[jj][ks] = lds_b128(ldsp(k < 2 ? cb[ks] : cb2[ks]) + (k & 1) * 32768 + jj * 8192);
  };
  auto z1_unpack = [&](auto kc) LAMBDA {
    constexpr int k = decltype(kc)::value;
#pragma unroll
    for (int jj = 0; jj < 2; ++jj) {
      hin[2 * k + jj][0] = unpack4(Xb[jj][0]);
      hin[2 * k + jj][1] = unpack4(Xb[jj][1]);
    }
  };
  auto z1_mma1 = [&](auto kc, auto ic) LAMBDA {
    constexpr int k = decltype(kc)::value, i = decltype(ic)::value;
    constexpr int ks = i >> 2, jj = (i >> 1) & 1, t = i & 1;
    hin[2 * k + jj][t] = mfma(A1f[ks][t], Cb[jj][ks], hin[2 * k + jj][t]);
  };
  auto z1_stats = [&](auto kc) LAMBDA {
    constexpr int k = decltype(kc)::value;
    float st[4];
#pragma unroll
    for (int jj = 0; jj < 2; ++jj) {
      const int j = 2 * k + jj;
      f32x2 a = (lo2(hin[j][0]) + hi2(hin[j][0])) + (lo2(hin[j][1]) + hi2(hin[j][1]));
      f32x2 b = lo2(hin[j][0]) * lo2(hin[j][0]);
      b = fma2(hi2(hin[j][0]), hi2(hin[j][0]), b);
      b = fma2(lo2(hin[j][1]), lo2(hin[j][1]), b);
      b = fma2(hi2(hin[j][1]), hi2(hin[j][1]), b);
      st[2 * jj] = a[0] + a[1];
      st[2 * jj + 1] = b[0] + b[1];
    }
    float r = reduce4(st[0], st[1], st[2], st[3]);
    *LDSP(float, ldsw(stw) + 2 * k * 256) = r;
  };
  f32x4 sp0, sp1;
  auto si_read = [&](auto kc) LAMBDA {
    constexpr int k = decltype(kc)::value;
    sp0 = *LDSP(const f32x4, ldsp(strh) + 2 * k * 256);
    sp1 = *LDSP(const f32x4, ldsp(strh) + 2 * k * 256 + 3072);
  };
  auto si_math = [&](auto kc, auto hook) LAMBDA {
    constexpr int k = decltype(kc)::value;
    float rs2[2], nb2[2];
    {
      f32x2 m = ((lo2(sp0) + hi2(sp0)) + (lo2(sp1) + hi2(sp1))) * bc2(1.f / 128.f);
      float var = __builtin_fmaxf(__builtin_fmaf(-m[0], m[0], m[1]), m[1] * 1e-6f);
      float r = __builtin_amdgcn_rsqf(var + 1e-5f) * -1.4426950408889634f;
      float n = -m[0] * r;
      auto a = __builtin_amdgcn_permlane32_swap(f2u(r), f2u(r), false, false);
      auto b = __builtin_amdgcn_permlane32_swap(f2u(n), f2u(n), false, false);
      rs2[0] = u2f(a[0]); rs2[1] = u2f(a[1]); nb2[0] = u2f(b[0]); nb2[1] = u2f(b[1]);
    }
    f32x2 t[8], e[8];
#pragma unroll
    for (int g = 0; g < 4; ++g) {
      t[2 * g]     = fma2(lo2(hin[2 * k + (g >> 1)][g & 1]), bc2(rs2[g >> 1]), bc2(nb2[g >> 1]));
      t[2 * g + 1] = fma2(hi2(hin[2 * k + (g >> 1)][g & 1]), bc2(rs2[g >> 1]), bc2(nb2[g >> 1]));
    }
    SCHED();
    static_for<0, 16>([&](auto ic) LAMBDA {
      constexpr int i = decltype(ic)::value;
      hook(ic);
      e[i >> 1][i & 1] = __builtin_amdgcn_exp2f(t[i >> 1][i & 1]);
      SCHED();
    });
    f32x2 P[4]; float rr[4];
#pragma unroll
    for (int g = 0; g < 4; ++g) { f32x2 g1 = e[2 * g] + bc2(1.f); P[g] = fma2(g1, e[2 * g + 1], g1); }
#pragma unroll
    for (int g = 0; g < 4; ++g) rr[g] = __builtin_amdgcn_rcpf(P[g][0] * P[g][1]);
#pragma unroll
    for (int g = 0; g < 4; ++g) { t[2 * g] = fma2(t[2 * g], e[2 * g + 1], t[2 * g]); t[2 * g + 1] = fma2(t[2 * g + 1], e[2 * g], t[2 * g + 1]); }
#pragma unroll
    for (int g = 0; g < 4; ++g) {
      f32x2 sw = __builtin_shufflevector(P[g], P[g], 1, 0);
      P[g] = sw * bc2(rr[g] * -0.6931471805599453f);
    }
#pragma unroll
    for (int g = 0; g < 4; ++g) {
      u32x2 pk = {pk2v(t[2 * g] * P[g]), pk2v(t[2 * g + 1] * P[g])};
      *LDSP(u32x2, ldsw(hw[2 * k + (g >> 1)]) + 2 * (g & 1) * PLANE) = pk;
    }
  };

  asm volatile("" :: "v"(aH0), "v"(aH1), "v"(aR0), "v"(aR1), "v"(aS0), "v"(aS1), "v"(hw[0]), "v"(hw[1]), "v"(hw[2]), "v"(hw[3]), "v"(hw[4]), "v"(hw[5]));
  asm volatile("" :: "v"(cb[0]), "v"(cb[1]), "v"(cb[2]), "v"(cb[3]), "v"(cb2[0]), "v"(cb2[1]), "v"(cb2[2]), "v"(cb2[3]), "v"(xb0), "v"(xb1), "v"(xb2), "v"(xb3), "v"(stw), "v"(strh));
  WAIT_VM(4);
  BAR();
#pragma unroll
  for (int ks = 0; ks < 4; ++ks) {
    A1f[ks][0] = lds_tr_frag(aptr(H_OFF + ks * 8192, 0));
    A1f[ks][1] = lds_tr_frag(aptr(H_OFF + ks * 8192, 1));
  }
  WAIT_VM(0);
  BAR();
  STAMP(1);
  z1_read(IC<0>{});
  SCHED();
  z1_unpack(IC<0>{});
  static_for<0, 16>([&](auto ic) LAMBDA { z1_mma1(IC<0>{}, ic); });
  z1_stats(IC<0>{});
  SCHED();
  BAR();
  STAMP(3);
  si_read(IC<0>{});
  z1_read(IC<1>{});
  SCHED();
  z1_unpack(IC<1>{});
  si_math(IC<0>{}, [&](auto ic) LAMBDA { z1_mma1(IC<1>{}, ic); });
  z1_stats(IC<1>{});
  SCHED();
  BAR();
  STAMP(5);
  si_read(IC<1>{});
  z1_read(IC<2>{});
  SCHED();
  z1_unpack(IC<2>{});
  si_math(IC<1>{}, [&](auto ic) LAMBDA { z1_mma1(IC<2>{}, ic); });
  z1_stats(IC<2>{});
  SCHED();
  BAR();
  STAMP(7);
  si_read(IC<2>{});
  s16x4 Ah[2][3][2][2];
#pragma unroll
  for (int di = 0; di < 3; ++di)
#pragma unroll
    for (int t = 0; t < 2; ++t) {
      Ah[0][di][t][0] = lds_tr(aptr(blk_off(0, di), t));
      Ah[0][di][t][1] = lds_tr(aptr(blk_off(0, di), t) + 1024);
    }
  SCHED();
  si_math(IC<2>{}, [&](auto) LAMBDA {});
  f32x4 acc[4][2];
#pragma unroll
  for (int i = 0; i < 4; ++i) { acc[i][0] = hin[i + 1][0]; acc[i][1] = hin[i + 1][1]; }
  SCHED();
  BAR();
  STAMP(9);

  const lds_char* hb = ldsp(lds0 + H_OFF + q * PLANE + (T * 4 * 18 + px) * 16);
  bf16x8 Bf[2][6];
#pragma unroll
  for (int rr = 0; rr < 6; ++rr) Bf[0][rr] = lds_b128(hb + rr * 18 * 16);
  const int lp = lane >> 2, lc = lane & 3;
  const unsigned xoff = (unsigned)(lp * 256 + (4 * w + (lc ^ ((lp >> 2) & 3))) * 16);
  const unsigned xsoff = (unsigned)(((bimg * 128 + ty0 + 4 * T) * 128 + tx0) * 256);
  const unsigned xrd0 = (unsigned)(px * 64 + (((q >> 1) ^ ((px >> 2) & 3)) * 16) + (q & 1) * 8);
  const unsigned xrd1 = (unsigned)(px * 64 + (((2 + (q >> 1)) ^ ((px >> 2) & 3)) * 16) + (q & 1) * 8);
  const unsigned xfl = lds0 + XF_OFF + wv * 2048;
  bf16x8 A2f[8], B2[4][2];
  f32x4 o2[4][2];
  u32x2 xq[4][2];
  const unsigned owbase = lds0 + (w < 3 ? R2 + w * 8192 : R1 + 8192) + (q >> 1) * HOUT_PLANE + (q & 1) * 8 + (4 * T * 16 + px) * 16;
  const unsigned hr = lds0 + q * HOUT_PLANE + (4 * T * 16 + px) * 16;
  const unsigned hr2 = hr + R2, hr1 = hr + R1 + 8192;
  auto put_hout = [&](auto rc) LAMBDA {
    constexpr int r = decltype(rc)::value;
    *LDSP(u32x2, ldsw(owbase) + r * 256) = pack4(acc[r][0]);
    *LDSP(u32x2, ldsw(owbase) + 2 * HOUT_PLANE + r * 256) = pack4(acc[r][1]);
  };
  auto b2 = [&](int ks, int i) LAMBDA -> bf16x8 { return lds_b128((ks < 3 ? ldsp(hr2) + ks * 8192 : ldsp(hr1)) + i * 256); };
  static_for<0, 12>([&](auto sc) LAMBDA {
    constexpr int s = decltype(sc)::value;
    constexpr int cur = s & 1, nxt = cur ^ 1;
    SCHED();
    if constexpr ((s & 1) == 1) BAR();
    SCHED();
    static_for<0, 24>([&](auto mc) LAMBDA {
      constexpr int m = decltype(mc)::value;
      constexpr int di = s < 11 ? m / 8 : (m % 6) / 2, i = s < 11 ? (m % 8) / 2 : m / 6, t = m & 1;
      acc[i][t] = mfma(join(Ah[cur][di][t][0], Ah[cur][di][t][1]), Bf[cur][i + di], acc[i][t]);
      if constexpr (s < 11) {
        constexpr int s1 = s + 1, dj = s1 >> 2, ks = s1 & 3;
        if constexpr (m < 12) {
          constexpr int rdi = m >> 2, rt = (m >> 1) & 1, rh = m & 1;
          Ah[nxt][rdi][rt][rh] = lds_tr(aptr(blk_off(s1, rdi), rt) + rh * 1024);
        } else if constexpr (m < 18) {
          constexpr int rr = m - 12;
          Bf[nxt][rr] = lds_b128(hb + ks * 4 * PLANE + (rr * 18 + dj) * 16);
        }
      }
      if constexpr ((s == 3 || s == 7) && (m == 4 || m == 12)) {
        constexpr int r = (s == 3 ? 0 : 2) + (m == 12 ? 1 : 0);
        dma16_nt(r_xf, xoff, xsoff + r * 32768, xfl + (r & 1) * 1024);
      }
      if constexpr ((s == 6 || s == 10) && m == 20) WAIT_VM(0);
      if constexpr ((s == 6 || s == 10) && m >= 20) {
        constexpr int r = (s == 6 ? 0 : 2) + ((m >> 1) & 1), tt = m & 1;
        xq[r][tt] = *LDSP(const u32x2, ldsp(xfl + (tt ? xrd1 : xrd0)) + (r & 1) * 1024);
      }
      if constexpr (s == 11) {
        if constexpr (m < 8) A2f[m] = lds_tr_frag(aptr(wz2_off(m >> 1), m & 1));
        if constexpr (m >= 8 && m < 16) { constexpr int e = m - 8; o2[e >> 1][e & 1] = unpack4(xq[e >> 1][e & 1]); }
        if constexpr (m == 8) put_hout(IC<0>{});
        if constexpr (m == 14) put_hout(IC<1>{});
        if constexpr (m == 16) BAR();
        if constexpr (m >= 16) { constexpr int e = m - 16; B2[e & 3][e >> 2] = b2(e & 3, e >> 2); }
        if constexpr (m == 20) put_hout(IC<2>{});
      }
      SCHED();
    });
  });
  put_hout(IC<3>{});
  SCHED();
  STAMP(10);
  BAR();
  STAMP(11);
  {
    bf16x8 B3[4][2];
#pragma unroll
    for (int i = 0; i < 2; ++i)
#pragma unroll
      for (int ks = 0; ks < 4; ++ks) B3[ks][i] = b2(ks, 2 + i);
    SCHED();
    const unsigned ost = lds0 + H_OFF + wv * 4096;
    const unsigned ow0 = ost + xrd0, ow1 = ost + xrd1, ord = ost + lane * 16;
    u32x4 ov[4];
    auto put = [&](auto ic) LAMBDA {
      constexpr int i = decltype(ic)::value;
      *LDSP(u32x2, ldsw(ow0) + i * 1024) = pack4(o2[i][0]);
      *LDSP(u32x2, ldsw(ow1) + i * 1024) = pack4(o2[i][1]);
      ov[i] = *LDSP(const u32x4, ldsp(ord) + i * 1024);
    };
    auto store = [&](auto ic) LAMBDA {
      constexpr int i = decltype(ic)::value;
      __builtin_amdgcn_raw_buffer_store_b128(ov[i], r_o, xoff, xsoff + i * 32768, STORE_AUX);
    };
    static_for<0, 16>([&](auto mc) LAMBDA {
      constexpr int m = decltype(mc)::value, i = m >> 2, ks = m & 3;
      bf16x8 B;
      if constexpr (i < 2) B = B2[ks][i]; else B = B3[ks][i - 2];
      o2[i][0] = mfma(A2f[2 * ks],     B, o2[i][0]);
      o2[i][1] = mfma(A2f[2 * ks + 1], B, o2[i][1]);
      if constexpr (m == 5) put(IC<0>{});
      if constexpr (m == 8) store(IC<0>{});
      if constexpr (m == 9) put(IC<1>{});
      if constexpr (m == 12) store(IC<1>{});
      if constexpr (m == 13) put(IC<2>{});
      SCHED();
    });
    store(IC<2>{});
    put(IC<3>{});
    store(IC<3>{});
  }
  STAMP(12);
}

extern "C" void kernel_launch(void* const* d_in, const int* in_sizes, int n_in,
                              void* d_out, int out_size, void* d_ws, size_t ws_size,
                              hipStream_t stream) {
  const __hip_bfloat16* xf  = (const __hip_bfloat16*)d_in[0];
  const __hip_bfloat16* x   = (const __hip_bfloat16*)d_in[1];
  const __hip_bfloat16* c   = (const __hip_bfloat16*)d_in[2];
  const __hip_bfloat16* wz1 = (const __hip_bfloat16*)d_in[3];
  const __hip_bfloat16* wz2 = (const __hip_bfloat16*)d_in[4];
  const __hip_bfloat16* wc  = (const __hip_bfloat16*)d_in[5];
  (void)hipFuncSetAttribute((const void*)controlnet_fused, hipFuncAttributeMaxDynamicSharedMemorySize, LDS_BYTES);
  controlnet_fused<<<256, NTHREADS, LDS_BYTES, stream>>>(xf, x, c, wz1, wz2, wc, (__hip_bfloat16*)d_out
  );
}
```

```cpp
#include <hip/hip_runtime.h>
#include <hip/hip_bf16.h>

typedef __attribute__((ext_vector_type(8))) __bf16 bf16x8;
typedef __attribute__((ext_vector_type(2))) __bf16 bf16x2;
typedef __attribute__((ext_vector_type(4))) short s16x4;
typedef __attribute__((ext_vector_type(8))) short s16x8;
typedef __attribute__((ext_vector_type(4))) float f32x4;
typedef __attribute__((ext_vector_type(2))) float f32x2;
typedef __attribute__((ext_vector_type(4))) unsigned u32x4;
typedef __attribute__((ext_vector_type(2))) unsigned u32x2;
typedef __attribute__((address_space(3))) char lds_char;
#define LDSP(T, p) ((__attribute__((address_space(3))) T*)(p))
#define DEVINL __device__ __forceinline__
#define LAMBDA __attribute__((always_inline))

constexpr int PLANE     = 3072;
constexpr int H_OFF     = 0;
constexpr int R0 = 49152, R1 = 81920, R2 = 114688;
constexpr int STATS_OFF = 147456;
constexpr int XF_OFF    = STATS_OFF;
constexpr int LDS_BYTES = STATS_OFF + 16384;
constexpr int HOUT_PLANE = 2048;
__host__ __device__ constexpr int reg_off(int a) { return a == 0 ? R0 : a == 1 ? R1 : R2; }
__host__ __device__ constexpr int blk_off(int s, int di) { return (s & 3) < 3 ? reg_off(s & 3) + di * 8192 : reg_off(di) + 24576; }
__host__ __device__ constexpr int wz2_off(int ks) { return ks < 3 ? R0 + ks * 8192 : R1; }
__host__ __device__ constexpr int hout_off(int ks) { return ks < 3 ? R2 + ks * 8192 : R1 + 8192; }
constexpr unsigned OOB  = 0x40000000u;
constexpr int TENSOR_BYTES = 2 * 128 * 128 * 128 * 2;
constexpr int NTHREADS = 576;

#define STAMP(i) do {} while (0)
#define STORE_AUX 17
#define WAIT_VM(n) asm volatile("s_waitcnt vmcnt(" #n ")" ::: "memory")
#define BAR() asm volatile("s_waitcnt lgkmcnt(0)\n\ts_barrier" ::: "memory")
#define SCHED() __builtin_amdgcn_sched_barrier(0)

template <int N> struct IC { static constexpr int value = N; };
template <int I, int N, class F> DEVINL void static_for(F&& f) { if constexpr (I < N) { f(IC<I>{}); static_for<I + 1, N>(f); } }

DEVINL u32x4 make_rsrc(const void* p, unsigned bytes) {
  unsigned long long a = (unsigned long long)p;
  u32x4 r = {(unsigned)a, (unsigned)(a >> 32) & 0xffffu, bytes, 0x00020000u};
  return r;
}
DEVINL void dma16(u32x4 rsrc, unsigned voff, unsigned soff, unsigned lds_addr) {
  asm volatile("s_mov_b32 m0, %0\n\ts_nop 0\n\tbuffer_load_dwordx4 %1, %2, %3 offen lds" :: "s"(lds_addr), "v"(voff), "s"(rsrc), "s"(soff) : "memory");
}
DEVINL void dma16_nt(u32x4 rsrc, unsigned voff, unsigned soff, unsigned lds_addr) {
  asm volatile("s_mov_b32 m0, %0\n\ts_nop 0\n\tbuffer_load_dwordx4 %1, %2, %3 offen nt lds" :: "s"(lds_addr), "v"(voff), "s"(rsrc), "s"(soff) : "memory");
}
DEVINL void dma16x4(u32x4 rsrc, unsigned v0, unsigned v1, unsigned v2, unsigned v3, unsigned soff, unsigned lds_addr) {
  asm volatile("s_mov_b32 m0, %0\n\ts_nop 0\n\t"
               "buffer_load_dwordx4 %1, %5, %6 offen lds\n\t"
               "buffer_load_dwordx4 %2, %5, %6 offen offset:1024 lds\n\t"
               "buffer_load_dwordx4 %3, %5, %6 offen offset:2048 lds\n\t"
               "buffer_load_dwordx4 %4, %5, %6 offen offset:3072 lds"
               :: "s"(lds_addr), "v"(v0), "v"(v1), "v"(v2), "v"(v3), "s"(rsrc), "s"(soff) : "memory");
}
DEVINL void dma16x4_nt(u32x4 rsrc, unsigned v0, unsigned v1, unsigned v2, unsigned v3, unsigned soff, unsigned lds_addr) {
  asm volatile("s_mov_b32 m0, %0\n\ts_nop 0\n\t"
               "buffer_load_dwordx4 %1, %5, %6 offen nt lds\n\t"
               "buffer_load_dwordx4 %2, %5, %6 offen offset:1024 nt lds\n\t"
               "buffer_load_dwordx4 %3, %5, %6 offen offset:2048 nt lds\n\t"
               "buffer_load_dwordx4 %4, %5, %6 offen offset:3072 nt lds"
               :: "s"(lds_addr), "v"(v0), "v"(v1), "v"(v2), "v"(v3), "s"(rsrc), "s"(soff) : "memory");
}
DEVINL const lds_char* ldsp(unsigned a) { return (const lds_char*)(size_t)a; }
DEVINL lds_char* ldsw(unsigned a) { return (lds_char*)(size_t)a; }
DEVINL bf16x8 lds_b128(const lds_char* p) { return *LDSP(const bf16x8, p); }
DEVINL s16x4 lds_tr(const lds_char* p) { return __builtin_amdgcn_ds_read_tr16_b64_v4i16(LDSP(s16x4, p)); }
DEVINL bf16x8 join(s16x4 lo, s16x4 hi) { return __builtin_bit_cast(bf16x8, __builtin_shufflevector(lo, hi, 0, 1, 2, 3, 4, 5, 6, 7)); }
DEVINL bf16x8 lds_tr_frag(const lds_char* p) { return join(lds_tr(p), lds_tr(p + 1024)); }
DEVINL f32x4 mfma(bf16x8 a, bf16x8 b, f32x4 c) { return __builtin_amdgcn_mfma_f32_16x16x32_bf16(a, b, c, 0, 0, 0); }
DEVINL unsigned pk2v(f32x2 f) { return __builtin_bit_cast(unsigned, __builtin_convertvector(f, bf16x2)); }
DEVINL f32x2 lo2(f32x4 v) { return __builtin_shufflevector(v, v, 0, 1); }
DEVINL f32x2 hi2(f32x4 v) { return __builtin_shufflevector(v, v, 2, 3); }
DEVINL u32x2 pack4(f32x4 v) { u32x2 r = {pk2v(lo2(v)), pk2v(hi2(v))}; return r; }
DEVINL float bf_lo(unsigned u) { return __builtin_bit_cast(float, u << 16); }
DEVINL float bf_hi(unsigned u) { return __builtin_bit_cast(float, u & 0xffff0000u); }
DEVINL unsigned f2u(float f) { return __builtin_bit_cast(unsigned, f); }
DEVINL float u2f(unsigned u) { return __builtin_bit_cast(float, u); }
DEVINL f32x4 unpack4(u32x2 v) { f32x4 r = {bf_lo(v[0]), bf_hi(v[0]), bf_lo(v[1]), bf_hi(v[1])}; return r; }
DEVINL f32x2 bc2(float v) { f32x2 r = {v, v}; return r; }
DEVINL f32x2 fma2(f32x2 a, f32x2 b, f32x2 c) { return __builtin_elementwise_fma(a, b, c); }
DEVINL float reduce4(float a, float b, float c, float d) {
  auto r0 = __builtin_amdgcn_permlane16_swap(f2u(a), f2u(b), false, false);
  auto r1 = __builtin_amdgcn_permlane16_swap(f2u(c), f2u(d), false, false);
  float ab = u2f(r0[0]) + u2f(r0[1]);
  float cd = u2f(r1[0]) + u2f(r1[1]);
  auto r2 = __builtin_amdgcn_permlane32_swap(f2u(ab), f2u(cd), false, false);
  return u2f(r2[0]) + u2f(r2[1]);
}

__global__ __launch_bounds__(NTHREADS, 3)
void controlnet_fused(const __hip_bfloat16* __restrict__ xf, const __hip_bfloat16* __restrict__ x,
                      const __hip_bfloat16* __restrict__ c, const __hip_bfloat16* __restrict__ wz1,
                      const __hip_bfloat16* __restrict__ wz2, const __hip_bfloat16* __restrict__ wconv,
                      __hip_bfloat16* __restrict__ out
                      ) {
  extern __shared__ __attribute__((aligned(16))) char smem_generic[];
  const unsigned lds0 = (unsigned)(size_t)(lds_char*)smem_generic;

  const int tid = threadIdx.x;
  const int lane = tid & 63;
  const int wv = __builtin_amdgcn_readfirstlane(tid >> 6);
  const int q = lane >> 4, px = lane & 15;
  const int bid = blockIdx.x;
  const int tile = (bid & 7) * 32 + (bid >> 3);
  const int bimg = tile >> 7, ty0 = ((tile >> 3) & 15) * 8, tx0 = (tile & 7) * 16;
  const u32x4 r_c  = make_rsrc(c, TENSOR_BYTES), r_x = make_rsrc(x, TENSOR_BYTES), r_xf = make_rsrc(xf, TENSOR_BYTES);

  auto grp_row = [&](int T, int j) LAMBDA -> int { return T == 0 ? j : (j == 0 ? 9 : j + 4); };
  auto row_soff = [&](int r) LAMBDA -> unsigned {
    const int gy = ty0 - 1 + r;
    return (unsigned)gy < 128u ? (unsigned)(((bimg * 128 + gy) * 128 + tx0) * 256) : OOB;
  };
  auto row_voff = [&](int w_) LAMBDA -> unsigned { const int p = 4 * w_ + q; return (unsigned)(p * 256 + ((px ^ p) * 16)); };
  auto col_voff = [&](int T, int w_) LAMBDA -> unsigned {
    const int p = 4 * w_ + q;
    const int gy = ty0 - 1 + 5 * T + (p >> 1), gx = tx0 - 1 + ((p & 1) ? 17 : 0);
    const bool ok = p < 10 && (unsigned)gy < 128u && (unsigned)gx < 128u;
    return ok ? (unsigned)(((bimg * 128 + gy) * 128 + gx) * 256 + ((px ^ p) * 16)) : OOB;
  };

  if (wv == 8) {
    unsigned rv[4];
#pragma unroll
    for (int w_ = 0; w_ < 4; ++w_) rv[w_] = row_voff(w_) - w_ * 1024;
    auto rows = [&](int j) LAMBDA {
#pragma unroll
      for (int T = 0; T < 2; ++T) {
        const unsigned so = row_soff(grp_row(T, j));
        const unsigned dst = lds0 + reg_off(j >> 1) + (T * 2 + (j & 1)) * 8192;
        dma16x4_nt(r_c, rv[0], rv[1], rv[2], rv[3], so, dst);
        dma16x4_nt(r_x, rv[0], rv[1], rv[2], rv[3], so, dst + 4096);
      }
    };
    unsigned tw[4];
    {
      const u32x4 r_w2t = make_rsrc(wz2, 32768), r_wct = make_rsrc(wconv, 9 * 32768);
      const unsigned sl = (unsigned)(bid >> 3);
      const unsigned o0 = sl * 9216 + lane * 128;
      const unsigned o1 = lane < 8 ? sl * 9216 + (64 + lane) * 128 : OOB;
      const unsigned o2 = lane < 8 ? sl * 1024 + lane * 128 : OOB;
      asm volatile("buffer_load_dword %0, %1, %2, 0 offen" : "=v"(tw[0]) : "v"(o0), "s"(r_wct) : "memory");
      asm volatile("buffer_load_dword %0, %1, %2, 0 offen" : "=v"(tw[1]) : "v"(o1), "s"(r_wct) : "memory");
      asm volatile("buffer_load_dword %0, %1, %2, 0 offen" : "=v"(tw[2]) : "v"(o2), "s"(r_w2t) : "memory");
      tw[3] = 0;
    }
    BAR();
    rows(2); rows(3);
    BAR();
    rows(4);
#pragma unroll
    for (int T = 0; T < 2; ++T) {
      unsigned cv[4];
#pragma unroll
      for (int w_ = 0; w_ < 4; ++w_) { unsigned o = col_voff(T, w_); cv[w_] = o == OOB ? OOB : o - w_ * 1024; }
      const unsigned dst = lds0 + R2 + (T * 2 + 1) * 8192;
      dma16x4_nt(r_c, cv[0], cv[1], cv[2], cv[3], 0, dst);
      dma16x4_nt(r_x, cv[0], cv[1], cv[2], cv[3], 0, dst + 4096);
    }
    const u32x4 r_w2 = make_rsrc(wz2, 32768), r_wc = make_rsrc(wconv, 9 * 32768);
    const unsigned vA = (unsigned)(q * 256 + ((px ^ (q << 1)) * 16));
    const unsigned vB = (unsigned)(q * 256 + ((px ^ ((q | 4) << 1)) * 16));
    auto block = [&](u32x4 rs, int src, int dst) LAMBDA {
      dma16x4(rs, vA, vA, vB, vB, src, lds0 + dst);
      dma16x4(rs, vA, vA, vB, vB, src + 4096, lds0 + dst + 4096);
    };
    auto stage = [&](int s) LAMBDA {
#pragma unroll
      for (int di = 0; di < 3; ++di) block(r_wc, (di * 3 + (s >> 2)) * 32768 + (s & 3) * 8192, blk_off(s, di));
    };
    WAIT_VM(32); BAR();
    stage(0);
    WAIT_VM(24); BAR();
    stage(1);
    WAIT_VM(24); BAR();
    stage(2); stage(3);
    WAIT_VM(48); BAR();
    stage(4);
    WAIT_VM(24); BAR();
    stage(5);
    WAIT_VM(0); BAR();
    const unsigned xlv = (unsigned)((lane >> 2) * 256 + (((lane & 3) ^ ((lane >> 4) & 3)) * 16));
    auto xf_rows = [&](int r0) LAMBDA {
#pragma unroll
      for (int v = 0; v < 8; ++v)
#pragma unroll
        for (int r = 0; r < 2; ++r)
          dma16_nt(r_xf, xlv, (unsigned)(((bimg * 128 + ty0 + 4 * (v >> 2) + r0 + r) * 128 + tx0) * 256 + (v & 3) * 64), lds0 + XF_OFF + v * 2048 + r * 1024);
    };
    stage(6); stage(7);
    xf_rows(0);
    WAIT_VM(16); BAR();
    stage(8); stage(9);
    WAIT_VM(0); BAR();
    stage(10); stage(11);
    WAIT_VM(0); BAR();
    block(r_w2, 0, wz2_off(0)); block(r_w2, 8192, wz2_off(1)); block(r_w2, 16384, wz2_off(2)); block(r_w2, 24576, wz2_off(3));
    xf_rows(2);
    WAIT_VM(16); BAR();
    WAIT_VM(0); BAR();
    asm volatile("" :: "v"(tw[0]), "v"(tw[1]), "v"(tw[2]), "v"(tw[3]));
    return;
  }

  const int T = wv >> 2, w = wv & 3;
  STAMP(0);
  {
    const u32x4 r_w1 = make_rsrc(wz1, 32768);
    const int wk = 4 * wv + q;
    const int wf = (wk & 3) | (((wk >> 3) & 1) << 2);
    const unsigned wvoff = (unsigned)(wk * 256 + ((px ^ (wf << 1)) * 16));
#pragma unroll
    for (int ks = 0; ks < 4; ++ks) dma16(r_w1, wvoff, ks * 8192, lds0 + wv * 1024 + H_OFF + ks * 8192);
    const unsigned rv = row_voff(w);
#pragma unroll
    for (int j = 0; j < 2; ++j) {
      const unsigned so = row_soff(grp_row(T, j));
      const unsigned dst = lds0 + R0 + (T * 2 + j) * 8192 + w * 1024;
      dma16_nt(r_c, rv, so, dst);
      dma16_nt(r_x, rv, so, dst + 4096);
    }
  }
  __amdgpu_buffer_rsrc_t r_o = __builtin_amdgcn_make_buffer_rsrc((void*)out, 0, TENSOR_BYTES, 0x00020000);
  const int aq = (lane & 15) >> 2, ap = lane & 3;
  const int af = aq | ((q & 1) << 2);
  const unsigned ab0 = (unsigned)((8 * q + aq) * 256 + (((2 * w) ^ af) * 32) + 8 * ap);
  const unsigned aH0 = lds0 + ab0, aH1 = lds0 + (ab0 ^ 32);
  const unsigned aR0 = aH0 + R0, aR1 = aH1 + R0;
  const unsigned aS0 = aH0 + R2, aS1 = aH1 + R2;
  auto aptr = [&](int off, int t) LAMBDA -> const lds_char* {
    if (off < R0) return ldsp(t ? aH1 : aH0) + off;
    if (off < R2) return ldsp(t ? aR1 : aR0) + (off - R0);
    return ldsp(t ? aS1 : aS0) + (off - R2);
  };
  unsigned hw[6];
  {
    const unsigned hwbase = lds0 + H_OFF + (4 * w + (q >> 1)) * PLANE + (q & 1) * 8;
#pragma unroll
    for (int j = 0; j < 5; ++j) hw[j] = hwbase + (grp_row(T, j) * 18 + px + 1) * 16;
    hw[5] = hwbase + (px < 10 ? (5 * T + (px >> 1)) * 18 + ((px & 1) ? 17 : 0) : 180 + 6 * T + (px - 10)) * 16;
  }
  unsigned cb[4], cb2[4];
#pragma unroll
  for (int ks = 0; ks < 4; ++ks) { cb[ks] = lds0 + R0 + T * 16384 + px * 256 + (((4 * ks + q) ^ px) * 16); cb2[ks] = cb[ks] + 65536; }
  const unsigned xb0 = lds0 + R0 + T * 16384 + 4096 + px * 256 + (((4 * w + (q >> 1)) ^ px) * 16) + (q & 1) * 8;
  const unsigned xb1 = lds0 + R0 + T * 16384 + 4096 + px * 256 + (((4 * w + 2 + (q >> 1)) ^ px) * 16) + (q & 1) * 8;
  const unsigned xb2 = xb0 + 65536, xb3 = xb1 + 65536;
  const unsigned stw = lds0 + STATS_OFF + (w >> 1) * 3072 + ((T * 6 + (q >> 1)) * 16 + px) * 16 + (w & 1) * 8 + (q & 1) * 4;
  const unsigned strh = lds0 + STATS_OFF + ((T * 6 + (q >> 1)) * 16 + px) * 16;

  f32x4 hin[6][2];
  bf16x8 A1f[4][2];
  bf16x8 Cb[2][4]; u32x2 Xb[2][2];
  auto z1_read = [&](auto kc) LAMBDA {
    constexpr int k = decltype(kc)::value;
#pragma unroll
    for (int jj = 0; jj < 2; ++jj) {
      Xb[jj][0] = *LDSP(const u32x2, ldsp(k < 2 ? xb0 : xb2) + (k & 1) * 32768 + jj * 8192);
      Xb[jj][1] = *LDSP(const u32x2, ldsp(k < 2 ? xb1 : xb3) + (k & 1) * 32768 + jj * 8192);
    }
#pragma unroll
    for (int ks = 0; ks < 4; ++ks)
#pragma unroll
      for (int jj = 0; jj < 2; ++jj) Cb[jj][ks] = lds_b128(ldsp(k < 2 ? cb[ks] : cb2[ks]) + (k & 1) * 32768 + jj * 8192);
  };
  auto z1_unpack = [&](auto kc) LAMBDA {
    constexpr int k = decltype(kc)::value;
#pragma unroll
    for (int jj = 0; jj < 2; ++jj) {
      hin[2 * k + jj][0] = unpack4(Xb[jj][0]);
      hin[2 * k + jj][1] = unpack4(Xb[jj][1]);
    }
  };
  auto z1_mma1 = [&](auto kc, auto ic) LAMBDA {
    constexpr int k = decltype(kc)::value, i = decltype(ic)::value;
    constexpr int ks = i >> 2, jj = (i >> 1) & 1, t = i & 1;
    hin[2 * k + jj][t] = mfma(A1f[ks][t], Cb[jj][ks], hin[2 * k + jj][t]);
  };
  auto z1_stats = [&](auto kc) LAMBDA {
    constexpr int k = decltype(kc)::value;
    float st[4];
#pragma unroll
    for (int jj = 0; jj < 2; ++jj) {
      const int j = 2 * k + jj;
      f32x2 a = (lo2(hin[j][0]) + hi2(hin[j][0])) + (lo2(hin[j][1]) + hi2(hin[j][1]));
      f32x2 b = lo2(hin[j][0]) * lo2(hin[j][0]);
      b = fma2(hi2(hin[j][0]), hi2(hin[j][0]), b);
      b = fma2(lo2(hin[j][1]), lo2(hin[j][1]), b);
      b = fma2(hi2(hin[j][1]), hi2(hin[j][1]), b);
      st[2 * jj] = a[0] + a[1];
      st[2 * jj + 1] = b[0] + b[1];
    }
    float r = reduce4(st[0], st[1], st[2], st[3]);
    *LDSP(float, ldsw(stw) + 2 * k * 256) = r;
  };
  f32x4 sp0, sp1;
  auto si_read = [&](auto kc) LAMBDA {
    constexpr int k = decltype(kc)::value;
    sp0 = *LDSP(const f32x4, ldsp(strh) + 2 * k * 256);
    sp1 = *LDSP(const f32x4, ldsp(strh) + 2 * k * 256 + 3072);
  };
  auto si_math = [&](auto kc, auto hook) LAMBDA {
    constexpr int k = decltype(kc)::value;
    float rs2[2], nb2[2];
    {
      f32x2 m = ((lo2(sp0) + hi2(sp0)) + (lo2(sp1) + hi2(sp1))) * bc2(1.f / 128.f);
      float var = __builtin_fmaxf(__builtin_fmaf(-m[0], m[0], m[1]), m[1] * 1e-6f);
      float r = __builtin_amdgcn_rsqf(var + 1e-5f) * -1.4426950408889634f;
      float n = -m[0] * r;
      auto a = __builtin_amdgcn_permlane32_swap(f2u(r), f2u(r), false, false);
      auto b = __builtin_amdgcn_permlane32_swap(f2u(n), f2u(n), false, false);
      rs2[0] = u2f(a[0]); rs2[1] = u2f(a[1]); nb2[0] = u2f(b[0]); nb2[1] = u2f(b[1]);
    }
    f32x2 t[8], e[8];
#pragma unroll
    for (int g = 0; g < 4; ++g) {
      t[2 * g]     = fma2(lo2(hin[2 * k + (g >> 1)][g & 1]), bc2(rs2[g >> 1]), bc2(nb2[g >> 1]));
      t[2 * g + 1] = fma2(hi2(hin[2 * k + (g >> 1)][g & 1]), bc2(rs2[g >> 1]), bc2(nb2[g >> 1]));
    }
    SCHED();
    static_for<0, 16>([&](auto ic) LAMBDA {
      constexpr int i = decltype(ic)::value;
      hook(ic);
      e[i >> 1][i & 1] = __builtin_amdgcn_exp2f(t[i >> 1][i & 1]);
      SCHED();
    });
    f32x2 P[4]; float rr[4];
#pragma unroll
    for (int g = 0; g < 4; ++g) { f32x2 g1 = e[2 * g] + bc2(1.f); P[g] = fma2(g1, e[2 * g + 1], g1); }
#pragma unroll
    for (int g = 0; g < 4; ++g) rr[g] = __builtin_amdgcn_rcpf(P[g][0] * P[g][1]);
#pragma unroll
    for (int g = 0; g < 4; ++g) { t[2 * g] = fma2(t[2 * g], e[2 * g + 1], t[2 * g]); t[2 * g + 1] = fma2(t[2 * g + 1], e[2 * g], t[2 * g + 1]); }
#pragma unroll
    for (int g = 0; g < 4; ++g) {
      f32x2 sw = __builtin_shufflevector(P[g], P[g], 1, 0);
      P[g] = sw * bc2(rr[g] * -0.6931471805599453f);
    }
#pragma unroll
    for (int g = 0; g < 4; ++g) {
      u32x2 pk = {pk2v(t[2 * g] * P[g]), pk2v(t[2 * g + 1] * P[g])};
      *LDSP(u32x2, ldsw(hw[2 * k + (g >> 1)]) + 2 * (g & 1) * PLANE) = pk;
    }
  };

  asm volatile("" :: "v"(aH0), "v"(aH1), "v"(aR0), "v"(aR1), "v"(aS0), "v"(aS1), "v"(hw[0]), "v"(hw[1]), "v"(hw[2]), "v"(hw[3]), "v"(hw[4]), "v"(hw[5]));
  asm volatile("" :: "v"(cb[0]), "v"(cb[1]), "v"(cb[2]), "v"(cb[3]), "v"(cb2[0]), "v"(cb2[1]), "v"(cb2[2]), "v"(cb2[3]), "v"(xb0), "v"(xb1), "v"(xb2), "v"(xb3), "v"(stw), "v"(strh));
  WAIT_VM(4);
  BAR();
#pragma unroll
  for (int ks = 0; ks < 4; ++ks) {
    A1f[ks][0] = lds_tr_frag(aptr(H_OFF + ks * 8192, 0));
    A1f[ks][1] = lds_tr_frag(aptr(H_OFF + ks * 8192, 1));
  }
  WAIT_VM(0);
  BAR();
  STAMP(1);
  z1_read(IC<0>{});
  SCHED();
  z1_unpack(IC<0>{});
  static_for<0, 16>([&](auto ic) LAMBDA { z1_mma1(IC<0>{}, ic); });
  z1_stats(IC<0>{});
  SCHED();
  BAR();
  STAMP(3);
  si_read(IC<0>{});
  z1_read(IC<1>{});
  SCHED();
  z1_unpack(IC<1>{});
  si_math(IC<0>{}, [&](auto ic) LAMBDA { z1_mma1(IC<1>{}, ic); });
  z1_stats(IC<1>{});
  SCHED();
  BAR();
  STAMP(5);
  si_read(IC<1>{});
  z1_read(IC<2>{});
  SCHED();
  z1_unpack(IC<2>{});
  si_math(IC<1>{}, [&](auto ic) LAMBDA { z1_mma1(IC<2>{}, ic); });
  z1_stats(IC<2>{});
  SCHED();
  BAR();
  STAMP(7);
  si_read(IC<2>{});
  s16x4 Ah[2][3][2][2];
#pragma unroll
  for (int di = 0; di < 3; ++di)
#pragma unroll
    for (int t = 0; t < 2; ++t) {
      Ah[0][di][t][0] = lds_tr(aptr(blk_off(0, di), t));
      Ah[0][di][t][1] = lds_tr(aptr(blk_off(0, di), t) + 1024);
    }
  SCHED();
  si_math(IC<2>{}, [&](auto) LAMBDA {});
  f32x4 acc[4][2];
#pragma unroll
  for (int i = 0; i < 4; ++i) { acc[i][0] = hin[i + 1][0]; acc[i][1] = hin[i + 1][1]; }
  SCHED();
  BAR();
  STAMP(9);

  const lds_char* hb = ldsp(lds0 + H_OFF + q * PLANE + (T * 4 * 18 + px) * 16);
  bf16x8 Bf[2][6];
#pragma unroll
  for (int rr = 0; rr < 6; ++rr) Bf[0][rr] = lds_b128(hb + rr * 18 * 16);
  const int lp = lane >> 2, lc = lane & 3;
  const unsigned xoff = (unsigned)(lp * 256 + (4 * w + (lc ^ ((lp >> 2) & 3))) * 16);
  const unsigned xsoff = (unsigned)(((bimg * 128 + ty0 + 4 * T) * 128 + tx0) * 256);
  const unsigned xrd0 = (unsigned)(px * 64 + (((q >> 1) ^ ((px >> 2) & 3)) * 16) + (q & 1) * 8);
  const unsigned xrd1 = (unsigned)(px * 64 + (((2 + (q >> 1)) ^ ((px >> 2) & 3)) * 16) + (q & 1) * 8);
  const unsigned xfl = lds0 + XF_OFF + wv * 2048;
  bf16x8 A2f[8], B2[4][2];
  f32x4 o2[4][2];
  u32x2 xq[4][2];
  const unsigned owbase = lds0 + (w < 3 ? R2 + w * 8192 : R1 + 8192) + (q >> 1) * HOUT_PLANE + (q & 1) * 8 + (4 * T * 16 + px) * 16;
  const unsigned hr = lds0 + q * HOUT_PLANE + (4 * T * 16 + px) * 16;
  const unsigned hr2 = hr + R2, hr1 = hr + R1 + 8192;
  auto put_hout = [&](auto rc) LAMBDA {
    constexpr int r = decltype(rc)::value;
    *LDSP(u32x2, ldsw(owbase) + r * 256) = pack4(acc[r][0]);
    *LDSP(u32x2, ldsw(owbase) + 2 * HOUT_PLANE + r * 256) = pack4(acc[r][1]);
  };
  auto b2 = [&](int ks, int i) LAMBDA -> bf16x8 { return lds_b128((ks < 3 ? ldsp(hr2) + ks * 8192 : ldsp(hr1)) + i * 256); };
  static_for<0, 12>([&](auto sc) LAMBDA {
    constexpr int s = decltype(sc)::value;
    constexpr int cur = s & 1, nxt = cur ^ 1;
    SCHED();
    if constexpr ((s & 1) == 1) BAR();
    SCHED();
    static_for<0, 24>([&](auto mc) LAMBDA {
      constexpr int m = decltype(mc)::value;
      constexpr int di = s < 11 ? m / 8 : (m % 6) / 2, i = s < 11 ? (m % 8) / 2 : m / 6, t = m & 1;
      acc[i][t] = mfma(join(Ah[cur][di][t][0], Ah[cur][di][t][1]), Bf[cur][i + di], acc[i][t]);
      if constexpr (s < 11) {
        constexpr int s1 = s + 1, dj = s1 >> 2, ks = s1 & 3;
        if constexpr (m < 12) {
          constexpr int rdi = m >> 2, rt = (m >> 1) & 1, rh = m & 1;
          Ah[nxt][rdi][rt][rh] = lds_tr(aptr(blk_off(s1, rdi), rt) + rh * 1024);
        } else if constexpr (m < 18) {
          constexpr int rr = m - 12;
          Bf[nxt][rr] = lds_b128(hb + ks * 4 * PLANE + (rr * 18 + dj) * 16);
        }
      }
      if constexpr (s == 8 && m >= 20) {
        constexpr int r = (m >> 1) & 1, tt = m & 1;
        xq[r][tt] = *LDSP(const u32x2, ldsp(xfl + (tt ? xrd1 : xrd0)) + r * 1024);
      }
      if constexpr (s == 11) {
        if constexpr (m < 8) A2f[m] = lds_tr_frag(aptr(wz2_off(m >> 1), m & 1));
        if constexpr (m >= 8 && m < 12) { constexpr int e = m - 8; o2[e >> 1][e & 1] = unpack4(xq[e >> 1][e & 1]); }
        if constexpr (m == 8) put_hout(IC<0>{});
        if constexpr (m == 14) put_hout(IC<1>{});
        if constexpr (m == 16) BAR();
        if constexpr (m >= 16) { constexpr int e = m - 16; B2[e & 3][e >> 2] = b2(e & 3, e >> 2); }
        if constexpr (m == 20) put_hout(IC<2>{});
        if constexpr (m == 17 || m == 18) {
          constexpr int r = m - 15;
          xq[r][0] = *LDSP(const u32x2, ldsp(xfl + xrd0) + (r & 1) * 1024);
          xq[r][1] = *LDSP(const u32x2, ldsp(xfl + xrd1) + (r & 1) * 1024);
        }
        if constexpr (m >= 20) { constexpr int e = m - 16; o2[e >> 1][e & 1] = unpack4(xq[e >> 1][e & 1]); }
      }
      SCHED();
    });
  });
  put_hout(IC<3>{});
  SCHED();
  STAMP(10);
  BAR();
  STAMP(11);
  {
    bf16x8 B3[4][2];
#pragma unroll
    for (int i = 0; i < 2; ++i)
#pragma unroll
      for (int ks = 0; ks < 4; ++ks) B3[ks][i] = b2(ks, 2 + i);
    SCHED();
    const unsigned ost = lds0 + H_OFF + wv * 4096;
    const unsigned ow0 = ost + xrd0, ow1 = ost + xrd1, ord = ost + lane * 16;
    u32x4 ov[4];
    auto put = [&](auto ic) LAMBDA {
      constexpr int i = decltype(ic)::value;
      *LDSP(u32x2, ldsw(ow0) + i * 1024) = pack4(o2[i][0]);
      *LDSP(u32x2, ldsw(ow1) + i * 1024) = pack4(o2[i][1]);
      ov[i] = *LDSP(const u32x4, ldsp(ord) + i * 1024);
    };
    auto store = [&](auto ic) LAMBDA {
      constexpr int i = decltype(ic)::value;
      __builtin_amdgcn_raw_buffer_store_b128(ov[i], r_o, xoff, xsoff + i * 32768, STORE_AUX);
    };
    static_for<0, 16>([&](auto mc) LAMBDA {
      constexpr int m = decltype(mc)::value, i = m >> 2, ks = m & 3;
      bf16x8 B;
      if constexpr (i < 2) B = B2[ks][i]; else B = B3[ks][i - 2];
      o2[i][0] = mfma(A2f[2 * ks],     B, o2[i][0]);
      o2[i][1] = mfma(A2f[2 * ks + 1], B, o2[i][1]);
      if constexpr (m == 5) put(IC<0>{});
      if constexpr (m == 8) store(IC<0>{});
      if constexpr (m == 9) put(IC<1>{});
      if constexpr (m == 12) store(IC<1>{});
      if constexpr (m == 13) put(IC<2>{});
      SCHED();
    });
    store(IC<2>{});
    put(IC<3>{});
    store(IC<3>{});
  }
  STAMP(12);
}

extern "C" void kernel_launch(void* const* d_in, const int* in_sizes, int n_in,
                              void* d_out, int out_size, void* d_ws, size_t ws_size,
                              hipStream_t stream) {
  const __hip_bfloat16* xf  = (const __hip_bfloat16*)d_in[0];
  const __hip_bfloat16* x   = (const __hip_bfloat16*)d_in[1];
  const __hip_bfloat16* c   = (const __hip_bfloat16*)d_in[2];
  const __hip_bfloat16* wz1 = (const __hip_bfloat16*)d_in[3];
  const __hip_bfloat16* wz2 = (const __hip_bfloat16*)d_in[4];
  const __hip_bfloat16* wc  = (const __hip_bfloat16*)d_in[5];
  (void)hipFuncSetAttribute((const void*)controlnet_fused, hipFuncAttributeMaxDynamicSharedMemorySize, LDS_BYTES);
  controlnet_fused<<<256, NTHREADS, LDS_BYTES, stream>>>(xf, x, c, wz1, wz2, wc, (__hip_bfloat16*)d_out
  );
}
```

```cpp
#include <hip/hip_runtime.h>
#include <hip/hip_bf16.h>

typedef __attribute__((ext_vector_type(8))) __bf16 bf16x8;
typedef __attribute__((ext_vector_type(2))) __bf16 bf16x2;
typedef __attribute__((ext_vector_type(4))) short s16x4;
typedef __attribute__((ext_vector_type(8))) short s16x8;
typedef __attribute__((ext_vector_type(4))) float f32x4;
typedef __attribute__((ext_vector_type(2))) float f32x2;
typedef __attribute__((ext_vector_type(4))) unsigned u32x4;
typedef __attribute__((ext_vector_type(2))) unsigned u32x2;
typedef __attribute__((address_space(3))) char lds_char;
#define LDSP(T, p) ((__attribute__((address_space(3))) T*)(p))
#define DEVINL __device__ __forceinline__
#define LAMBDA __attribute__((always_inline))

constexpr int PLANE     = 3072;
constexpr int H_OFF     = 0;
constexpr int R0 = 49152, R1 = 81920, R2 = 114688;
constexpr int STATS_OFF = 147456;
constexpr int XF_OFF    = STATS_OFF;
constexpr int LDS_BYTES = STATS_OFF + 16384;
constexpr int HOUT_PLANE = 2048;
__host__ __device__ constexpr int reg_off(int a) { return a == 0 ? R0 : a == 1 ? R1 : R2; }
__host__ __device__ constexpr int blk_off(int s, int di) { return (s & 3) < 3 ? reg_off(s & 3) + di * 8192 : reg_off(di) + 24576; }
__host__ __device__ constexpr int wz2_off(int ks) { return ks < 3 ? R0 + ks * 8192 : R1; }
__host__ __device__ constexpr int hout_off(int ks) { return ks < 3 ? R2 + ks * 8192 : R1 + 8192; }
constexpr unsigned OOB  = 0x40000000u;
constexpr int TENSOR_BYTES = 2 * 128 * 128 * 128 * 2;
constexpr int NTHREADS = 576;

constexpr int STORE_AUX = 17;
#define WAIT_VM(n) asm volatile("s_waitcnt vmcnt(" #n ")" ::: "memory")
#define BAR() asm volatile("s_waitcnt lgkmcnt(0)\n\ts_barrier" ::: "memory")
#define SCHED() __builtin_amdgcn_sched_barrier(0)

template <int N> struct IC { static constexpr int value = N; };
template <int I, int N, class F> DEVINL void static_for(F&& f) { if constexpr (I < N) { f(IC<I>{}); static_for<I + 1, N>(f); } }

DEVINL u32x4 make_rsrc(const void* p, unsigned bytes) {
  unsigned long long a = (unsigned long long)p;
  u32x4 r = {(unsigned)a, (unsigned)(a >> 32) & 0xffffu, bytes, 0x00020000u};
  return r;
}
DEVINL void dma16(u32x4 rsrc, unsigned voff, unsigned soff, unsigned lds_addr) {
  asm volatile("s_mov_b32 m0, %0\n\ts_nop 0\n\tbuffer_load_dwordx4 %1, %2, %3 offen lds" :: "s"(lds_addr), "v"(voff), "s"(rsrc), "s"(soff) : "memory");
}
DEVINL void dma16_nt(u32x4 rsrc, unsigned voff, unsigned soff, unsigned lds_addr) {
  asm volatile("s_mov_b32 m0, %0\n\ts_nop 0\n\tbuffer_load_dwordx4 %1, %2, %3 offen nt lds" :: "s"(lds_addr), "v"(voff), "s"(rsrc), "s"(soff) : "memory");
}
DEVINL void dma16x4(u32x4 rsrc, unsigned v0, unsigned v1, unsigned v2, unsigned v3, unsigned soff, unsigned lds_addr) {
  asm volatile("s_mov_b32 m0, %0\n\ts_nop 0\n\t"
               "buffer_load_dwordx4 %1, %5, %6 offen lds\n\t"
               "buffer_load_dwordx4 %2, %5, %6 offen offset:1024 lds\n\t"
               "buffer_load_dwordx4 %3, %5, %6 offen offset:2048 lds\n\t"
               "buffer_load_dwordx4 %4, %5, %6 offen offset:3072 lds"
               :: "s"(lds_addr), "v"(v0), "v"(v1), "v"(v2), "v"(v3), "s"(rsrc), "s"(soff) : "memory");
}
DEVINL void dma16x4_nt(u32x4 rsrc, unsigned v0, unsigned v1, unsigned v2, unsigned v3, unsigned soff, unsigned lds_addr) {
  asm volatile("s_mov_b32 m0, %0\n\ts_nop 0\n\t"
               "buffer_load_dwordx4 %1, %5, %6 offen nt lds\n\t"
               "buffer_load_dwordx4 %2, %5, %6 offen offset:1024 nt lds\n\t"
               "buffer_load_dwordx4 %3, %5, %6 offen offset:2048 nt lds\n\t"
               "buffer_load_dwordx4 %4, %5, %6 offen offset:3072 nt lds"
               :: "s"(lds_addr), "v"(v0), "v"(v1), "v"(v2), "v"(v3), "s"(rsrc), "s"(soff) : "memory");
}
DEVINL const lds_char* ldsp(unsigned a) { return (const lds_char*)(size_t)a; }
DEVINL lds_char* ldsw(unsigned a) { return (lds_char*)(size_t)a; }
DEVINL bf16x8 lds_b128(const lds_char* p) { return *LDSP(const bf16x8, p); }
DEVINL s16x4 lds_tr(const lds_char* p) { return __builtin_amdgcn_ds_read_tr16_b64_v4i16(LDSP(s16x4, p)); }
DEVINL bf16x8 join(s16x4 lo, s16x4 hi) { return __builtin_bit_cast(bf16x8, __builtin_shufflevector(lo, hi, 0, 1, 2, 3, 4, 5, 6, 7)); }
DEVINL bf16x8 lds_tr_frag(const lds_char* p) { return join(lds_tr(p), lds_tr(p + 1024)); }
DEVINL f32x4 mfma(bf16x8 a, bf16x8 b, f32x4 c) { return __builtin_amdgcn_mfma_f32_16x16x32_bf16(a, b, c, 0, 0, 0); }
DEVINL unsigned pk2v(f32x2 f) { return __builtin_bit_cast(unsigned, __builtin_convertvector(f, bf16x2)); }
DEVINL f32x2 lo2(f32x4 v) { return __builtin_shufflevector(v, v, 0, 1); }
DEVINL f32x2 hi2(f32x4 v) { return __builtin_shufflevector(v, v, 2, 3); }
DEVINL u32x2 pack4(f32x4 v) { u32x2 r = {pk2v(lo2(v)), pk2v(hi2(v))}; return r; }
DEVINL float bf_lo(unsigned u) { return __builtin_bit_cast(float, u << 16); }
DEVINL float bf_hi(unsigned u) { return __builtin_bit_cast(float, u & 0xffff0000u); }
DEVINL unsigned f2u(float f) { return __builtin_bit_cast(unsigned, f); }
DEVINL float u2f(unsigned u) { return __builtin_bit_cast(float, u); }
DEVINL f32x4 unpack4(u32x2 v) { f32x4 r = {bf_lo(v[0]), bf_hi(v[0]), bf_lo(v[1]), bf_hi(v[1])}; return r; }
DEVINL f32x2 bc2(float v) { f32x2 r = {v, v}; return r; }
DEVINL f32x2 fma2(f32x2 a, f32x2 b, f32x2 c) { return __builtin_elementwise_fma(a, b, c); }
DEVINL float reduce4(float a, float b, float c, float d) {
  auto r0 = __builtin_amdgcn_permlane16_swap(f2u(a), f2u(b), false, false);
  auto r1 = __builtin_amdgcn_permlane16_swap(f2u(c), f2u(d), false, false);
  float ab = u2f(r0[0]) + u2f(r0[1]);
  float cd = u2f(r1[0]) + u2f(r1[1]);
  auto r2 = __builtin_amdgcn_permlane32_swap(f2u(ab), f2u(cd), false, false);
  return u2f(r2[0]) + u2f(r2[1]);
}

__global__ __launch_bounds__(NTHREADS, 3)
void controlnet_fused(const __hip_bfloat16* __restrict__ xf, const __hip_bfloat16* __restrict__ x,
                      const __hip_bfloat16* __restrict__ c, const __hip_bfloat16* __restrict__ wz1,
                      const __hip_bfloat16* __restrict__ wz2, const __hip_bfloat16* __restrict__ wconv,
                      __hip_bfloat16* __restrict__ out) {
  extern __shared__ __attribute__((aligned(16))) char smem_generic[];
  const unsigned lds0 = (unsigned)(size_t)(lds_char*)smem_generic;

  const int tid = threadIdx.x;
  const int lane = tid & 63;
  const int wv = __builtin_amdgcn_readfirstlane(tid >> 6);
  const int q = lane >> 4, px = lane & 15;
  const int bid = blockIdx.x;
  const int tile = (bid & 7) * 32 + (bid >> 3);
  const int bimg = tile >> 7, ty0 = ((tile >> 3) & 15) * 8, tx0 = (tile & 7) * 16;
  const u32x4 r_c  = make_rsrc(c, TENSOR_BYTES), r_x = make_rsrc(x, TENSOR_BYTES), r_xf = make_rsrc(xf, TENSOR_BYTES);

  auto grp_row = [&](int T, int j) LAMBDA -> int { return T == 0 ? j : (j == 0 ? 9 : j + 4); };
  auto row_soff = [&](int r) LAMBDA -> unsigned {
    const int gy = ty0 - 1 + r;
    return (unsigned)gy < 128u ? (unsigned)(((bimg * 128 + gy) * 128 + tx0) * 256) : OOB;
  };
  auto row_voff = [&](int w_) LAMBDA -> unsigned { const int p = 4 * w_ + q; return (unsigned)(p * 256 + ((px ^ p) * 16)); };
  auto col_voff = [&](int T, int w_) LAMBDA -> unsigned {
    const int p = 4 * w_ + q;
    const int gy = ty0 - 1 + 5 * T + (p >> 1), gx = tx0 - 1 + ((p & 1) ? 17 : 0);
    const bool ok = p < 10 && (unsigned)gy < 128u && (unsigned)gx < 128u;
    return ok ? (unsigned)(((bimg * 128 + gy) * 128 + gx) * 256 + ((px ^ p) * 16)) : OOB;
  };

  if (wv == 8) {
    unsigned rv[4];
#pragma unroll
    for (int w_ = 0; w_ < 4; ++w_) rv[w_] = row_voff(w_) - w_ * 1024;
    auto rows = [&](int j) LAMBDA {
#pragma unroll
      for (int T = 0; T < 2; ++T) {
        const unsigned so = row_soff(grp_row(T, j));
        const unsigned dst = lds0 + reg_off(j >> 1) + (T * 2 + (j & 1)) * 8192;
        dma16x4_nt(r_c, rv[0], rv[1], rv[2], rv[3], so, dst);
        dma16x4_nt(r_x, rv[0], rv[1], rv[2], rv[3], so, dst + 4096);
      }
    };
    unsigned tw[4];
    {
      const u32x4 r_w2t = make_rsrc(wz2, 32768), r_wct = make_rsrc(wconv, 9 * 32768);
      const unsigned sl = (unsigned)(bid >> 3);
      const unsigned o0 = sl * 9216 + lane * 128;
      const unsigned o1 = lane < 8 ? sl * 9216 + (64 + lane) * 128 : OOB;
      const unsigned o2 = lane < 8 ? sl * 1024 + lane * 128 : OOB;
      asm volatile("buffer_load_dword %0, %1, %2, 0 offen" : "=v"(tw[0]) : "v"(o0), "s"(r_wct) : "memory");
      asm volatile("buffer_load_dword %0, %1, %2, 0 offen" : "=v"(tw[1]) : "v"(o1), "s"(r_wct) : "memory");
      asm volatile("buffer_load_dword %0, %1, %2, 0 offen" : "=v"(tw[2]) : "v"(o2), "s"(r_w2t) : "memory");
      tw[3] = 0;
    }
    BAR();
    rows(2); rows(3);
    BAR();
    rows(4);
#pragma unroll
    for (int T = 0; T < 2; ++T) {
      unsigned cv[4];
#pragma unroll
      for (int w_ = 0; w_ < 4; ++w_) { unsigned o = col_voff(T, w_); cv[w_] = o == OOB ? OOB : o - w_ * 1024; }
      const unsigned dst = lds0 + R2 + (T * 2 + 1) * 8192;
      dma16x4_nt(r_c, cv[0], cv[1], cv[2], cv[3], 0, dst);
      dma16x4_nt(r_x, cv[0], cv[1], cv[2], cv[3], 0, dst + 4096);
    }
    const u32x4 r_w2 = make_rsrc(wz2, 32768), r_wc = make_rsrc(wconv, 9 * 32768);
    const unsigned vA = (unsigned)(q * 256 + ((px ^ (q << 1)) * 16));
    const unsigned vB = (unsigned)(q * 256 + ((px ^ ((q | 4) << 1)) * 16));
    auto block = [&](u32x4 rs, int src, int dst) LAMBDA {
      dma16x4(rs, vA, vA, vB, vB, src, lds0 + dst);
      dma16x4(rs, vA, vA, vB, vB, src + 4096, lds0 + dst + 4096);
    };
    auto stage = [&](int s) LAMBDA {
#pragma unroll
      for (int di = 0; di < 3; ++di) block(r_wc, (di * 3 + (s >> 2)) * 32768 + (s & 3) * 8192, blk_off(s, di));
    };
    WAIT_VM(32); BAR();
    stage(0);
    WAIT_VM(24); BAR();
    stage(1);
    WAIT_VM(24); BAR();
    stage(2); stage(3);
    WAIT_VM(48); BAR();
    stage(4);
    WAIT_VM(24); BAR();
    stage(5);
    WAIT_VM(0); BAR();
    const unsigned xlv = (unsigned)((lane >> 2) * 256 + (((lane & 3) ^ ((lane >> 4) & 3)) * 16));
    auto xf_rows = [&](int r0) LAMBDA {
#pragma unroll
      for (int v = 0; v < 8; ++v)
#pragma unroll
        for (int r = 0; r < 2; ++r)
          dma16_nt(r_xf, xlv, (unsigned)(((bimg * 128 + ty0 + 4 * (v >> 2) + r0 + r) * 128 + tx0) * 256 + (v & 3) * 64), lds0 + XF_OFF + v * 2048 + r * 1024);
    };
    stage(6); stage(7);
    xf_rows(0);
    WAIT_VM(16); BAR();
    stage(8); stage(9);
    WAIT_VM(0); BAR();
    stage(10); stage(11);
    WAIT_VM(0); BAR();
    block(r_w2, 0, wz2_off(0)); block(r_w2, 8192, wz2_off(1)); block(r_w2, 16384, wz2_off(2)); block(r_w2, 24576, wz2_off(3));
    xf_rows(2);
    WAIT_VM(16); BAR();
    WAIT_VM(0); BAR();
    asm volatile("" :: "v"(tw[0]), "v"(tw[1]), "v"(tw[2]), "v"(tw[3]));
    return;
  }

  const int T = wv >> 2, w = wv & 3;
  {
    const u32x4 r_w1 = make_rsrc(wz1, 32768);
    const int wk = 4 * wv + q;
    const int wf = (wk & 3) | (((wk >> 3) & 1) << 2);
    const unsigned wvoff = (unsigned)(wk * 256 + ((px ^ (wf << 1)) * 16));
#pragma unroll
    for (int ks = 0; ks < 4; ++ks) dma16(r_w1, wvoff, ks * 8192, lds0 + wv * 1024 + H_OFF + ks * 8192);
    const unsigned rv = row_voff(w);
#pragma unroll
    for (int j = 0; j < 2; ++j) {
      const unsigned so = row_soff(grp_row(T, j));
      const unsigned dst = lds0 + R0 + (T * 2 + j) * 8192 + w * 1024;
      dma16_nt(r_c, rv, so, dst);
      dma16_nt(r_x, rv, so, dst + 4096);
    }
  }
  __amdgpu_buffer_rsrc_t r_o = __builtin_amdgcn_make_buffer_rsrc((void*)out, 0, TENSOR_BYTES, 0x00020000);
  const int aq = (lane & 15) >> 2, ap = lane & 3;
  const int af = aq | ((q & 1) << 2);
  const unsigned ab0 = (unsigned)((8 * q + aq) * 256 + (((2 * w) ^ af) * 32) + 8 * ap);
  const unsigned aH0 = lds0 + ab0, aH1 = lds0 + (ab0 ^ 32);
  const unsigned aR0 = aH0 + R0, aR1 = aH1 + R0;
  const unsigned aS0 = aH0 + R2, aS1 = aH1 + R2;
  auto aptr = [&](int off, int t) LAMBDA -> const lds_char* {
    if (off < R0) return ldsp(t ? aH1 : aH0) + off;
    if (off < R2) return ldsp(t ? aR1 : aR0) + (off - R0);
    return ldsp(t ? aS1 : aS0) + (off - R2);
  };
  unsigned hw[6];
  {
    const unsigned hwbase = lds0 + H_OFF + (4 * w + (q >> 1)) * PLANE + (q & 1) * 8;
#pragma unroll
    for (int j = 0; j < 5; ++j) hw[j] = hwbase + (grp_row(T, j) * 18 + px + 1) * 16;
    hw[5] = hwbase + (px < 10 ? (5 * T + (px >> 1)) * 18 + ((px & 1) ? 17 : 0) : 180 + 6 * T + (px - 10)) * 16;
  }
  unsigned cb[4], cb2[4];
#pragma unroll
  for (int ks = 0; ks < 4; ++ks) { cb[ks] = lds0 + R0 + T * 16384 + px * 256 + (((4 * ks + q) ^ px) * 16); cb2[ks] = cb[ks] + 65536; }
  const unsigned xb0 = lds0 + R0 + T * 16384 + 4096 + px * 256 + (((4 * w + (q >> 1)) ^ px) * 16) + (q & 1) * 8;
  const unsigned xb1 = lds0 + R0 + T * 16384 + 4096 + px * 256 + (((4 * w + 2 + (q >> 1)) ^ px) * 16) + (q & 1) * 8;
  const unsigned xb2 = xb0 + 65536, xb3 = xb1 + 65536;
  const unsigned stw = lds0 + STATS_OFF + (w >> 1) * 3072 + ((T * 6 + (q >> 1)) * 16 + px) * 16 + (w & 1) * 8 + (q & 1) * 4;
  const unsigned strh = lds0 + STATS_OFF + ((T * 6 + (q >> 1)) * 16 + px) * 16;

  f32x4 hin[6][2];
  bf16x8 A1f[4][2];
  bf16x8 Cb[2][4]; u32x2 Xb[2][2];
  auto z1_read = [&](auto kc) LAMBDA {
    constexpr int k = decltype(kc)::value;
#pragma unroll
    for (int jj = 0; jj < 2; ++jj) {
      Xb[jj][0] = *LDSP(const u32x2, ldsp(k < 2 ? xb0 : xb2) + (k & 1) * 32768 + jj * 8192);
      Xb[jj][1] = *LDSP(const u32x2, ldsp(k < 2 ? xb1 : xb3) + (k & 1) * 32768 + jj * 8192);
    }
#pragma unroll
    for (int ks = 0; ks < 4; ++ks)
#pragma unroll
      for (int jj = 0; jj < 2; ++jj) Cb[jj][ks] = lds_b128(ldsp(k < 2 ? cb[ks] : cb2[ks]) + (k & 1) * 32768 + jj * 8192);
  };
  auto z1_unpack = [&](auto kc) LAMBDA {
    constexpr int k = decltype(kc)::value;
#pragma unroll
    for (int jj = 0; jj < 2; ++jj) {
      hin[2 * k + jj][0] = unpack4(Xb[jj][0]);
      hin[2 * k + jj][1] = unpack4(Xb[jj][1]);
    }
  };
  auto z1_mma1 = [&](auto kc, auto ic) LAMBDA {
    constexpr int k = decltype(kc)::value, i = decltype(ic)::value;
    constexpr int ks = i >> 2, jj = (i >> 1) & 1, t = i & 1;
    hin[2 * k + jj][t] = mfma(A1f[ks][t], Cb[jj][ks], hin[2 * k + jj][t]);
  };
  auto z1_stats = [&](auto kc) LAMBDA {
    constexpr int k = decltype(kc)::value;
    float st[4];
#pragma unroll
    for (int jj = 0; jj < 2; ++jj) {
      const int j = 2 * k + jj;
      f32x2 a = (lo2(hin[j][0]) + hi2(hin[j][0])) + (lo2(hin[j][1]) + hi2(hin[j][1]));
      f32x2 b = lo2(hin[j][0]) * lo2(hin[j][0]);
      b = fma2(hi2(hin[j][0]), hi2(hin[j][0]), b);
      b = fma2(lo2(hin[j][1]), lo2(hin[j][1]), b);
      b = fma2(hi2(hin[j][1]), hi2(hin[j][1]), b);
      st[2 * jj] = a[0] + a[1];
      st[2 * jj + 1] = b[0] + b[1];
    }
    float r = reduce4(st[0], st[1], st[2], st[3]);
    *LDSP(float, ldsw(stw) + 2 * k * 256) = r;
  };
  f32x4 sp0, sp1;
  auto si_read = [&](auto kc) LAMBDA {
    constexpr int k = decltype(kc)::value;
    sp0 = *LDSP(const f32x4, ldsp(strh) + 2 * k * 256);
    sp1 = *LDSP(const f32x4, ldsp(strh) + 2 * k * 256 + 3072);
  };
  auto si_math = [&](auto kc, auto hook) LAMBDA {
    constexpr int k = decltype(kc)::value;
    float rs2[2], nb2[2];
    {
      f32x2 m = ((lo2(sp0) + hi2(sp0)) + (lo2(sp1) + hi2(sp1))) * bc2(1.f / 128.f);
      float var = __builtin_fmaxf(__builtin_fmaf(-m[0], m[0], m[1]), m[1] * 1e-6f);
      float r = __builtin_amdgcn_rsqf(var + 1e-5f) * -1.4426950408889634f;
      float n = -m[0] * r;
      auto a = __builtin_amdgcn_permlane32_swap(f2u(r), f2u(r), false, false);
      auto b = __builtin_amdgcn_permlane32_swap(f2u(n), f2u(n), false, false);
      rs2[0] = u2f(a[0]); rs2[1] = u2f(a[1]); nb2[0] = u2f(b[0]); nb2[1] = u2f(b[1]);
    }
    f32x2 t[8], e[8];
#pragma unroll
    for (int g = 0; g < 4; ++g) {
      t[2 * g]     = fma2(lo2(hin[2 * k + (g >> 1)][g & 1]), bc2(rs2[g >> 1]), bc2(nb2[g >> 1]));
      t[2 * g + 1] = fma2(hi2(hin[2 * k + (g >> 1)][g & 1]), bc2(rs2[g >> 1]), bc2(nb2[g >> 1]));
    }
    SCHED();
    static_for<0, 16>([&](auto ic) LAMBDA {
      constexpr int i = decltype(ic)::value;
      hook(ic);
      e[i >> 1][i & 1] = __builtin_amdgcn_exp2f(t[i >> 1][i & 1]);
      SCHED();
    });
    f32x2 P[4]; float rr[4];
#pragma unroll
    for (int g = 0; g < 4; ++g) { f32x2 g1 = e[2 * g] + bc2(1.f); P[g] = fma2(g1, e[2 * g + 1], g1); }
#pragma unroll
    for (int g = 0; g < 4; ++g) rr[g] = __builtin_amdgcn_rcpf(P[g][0] * P[g][1]);
#pragma unroll
    for (int g = 0; g < 4; ++g) { t[2 * g] = fma2(t[2 * g], e[2 * g + 1], t[2 * g]); t[2 * g + 1] = fma2(t[2 * g + 1], e[2 * g], t[2 * g + 1]); }
#pragma unroll
    for (int g = 0; g < 4; ++g) {
      f32x2 sw = __builtin_shufflevector(P[g], P[g], 1, 0);
      P[g] = sw * bc2(rr[g] * -0.6931471805599453f);
    }
#pragma unroll
    for (int g = 0; g < 4; ++g) {
      u32x2 pk = {pk2v(t[2 * g] * P[g]), pk2v(t[2 * g + 1] * P[g])};
      *LDSP(u32x2, ldsw(hw[2 * k + (g >> 1)]) + 2 * (g & 1) * PLANE) = pk;
    }
  };

  asm volatile("" :: "v"(aH0), "v"(aH1), "v"(aR0), "v"(aR1), "v"(aS0), "v"(aS1), "v"(hw[0]), "v"(hw[1]), "v"(hw[2]), "v"(hw[3]), "v"(hw[4]), "v"(hw[5]));
  asm volatile("" :: "v"(cb[0]), "v"(cb[1]), "v"(cb[2]), "v"(cb[3]), "v"(cb2[0]), "v"(cb2[1]), "v"(cb2[2]), "v"(cb2[3]), "v"(xb0), "v"(xb1), "v"(xb2), "v"(xb3), "v"(stw), "v"(strh));
  WAIT_VM(4);
  BAR();
#pragma unroll
  for (int ks = 0; ks < 4; ++ks) {
    A1f[ks][0] = lds_tr_frag(aptr(H_OFF + ks * 8192, 0));
    A1f[ks][1] = lds_tr_frag(aptr(H_OFF + ks * 8192, 1));
  }
  WAIT_VM(0);
  BAR();
  z1_read(IC<0>{});
  SCHED();
  z1_unpack(IC<0>{});
  static_for<0, 16>([&](auto ic) LAMBDA { z1_mma1(IC<0>{}, ic); });
  z1_stats(IC<0>{});
  SCHED();
  BAR();
  si_read(IC<0>{});
  z1_read(IC<1>{});
  SCHED();
  z1_unpack(IC<1>{});
  si_math(IC<0>{}, [&](auto ic) LAMBDA { z1_mma1(IC<1>{}, ic); });
  z1_stats(IC<1>{});
  SCHED();
  BAR();
  si_read(IC<1>{});
  z1_read(IC<2>{});
  SCHED();
  z1_unpack(IC<2>{});
  si_math(IC<1>{}, [&](auto ic) LAMBDA { z1_mma1(IC<2>{}, ic); });
  z1_stats(IC<2>{});
  SCHED();
  BAR();
  si_read(IC<2>{});
  s16x4 Ah[2][3][2][2];
#pragma unroll
  for (int di = 0; di < 3; ++di)
#pragma unroll
    for (int t = 0; t < 2; ++t) {
      Ah[0][di][t][0] = lds_tr(aptr(blk_off(0, di), t));
      Ah[0][di][t][1] = lds_tr(aptr(blk_off(0, di), t) + 1024);
    }
  SCHED();
  si_math(IC<2>{}, [&](auto) LAMBDA {});
  f32x4 acc[4][2];
#pragma unroll
  for (int i = 0; i < 4; ++i) { acc[i][0] = hin[i + 1][0]; acc[i][1] = hin[i + 1][1]; }
  SCHED();
  BAR();

  const lds_char* hb = ldsp(lds0 + H_OFF + q * PLANE + (T * 4 * 18 + px) * 16);
  bf16x8 Bf[2][6];
#pragma unroll
  for (int rr = 0; rr < 6; ++rr) Bf[0][rr] = lds_b128(hb + rr * 18 * 16);
  const int lp = lane >> 2, lc = lane & 3;
  const unsigned xoff = (unsigned)(lp * 256 + (4 * w + (lc ^ ((lp >> 2) & 3))) * 16);
  const unsigned xsoff = (unsigned)(((bimg * 128 + ty0 + 4 * T) * 128 + tx0) * 256);
  const unsigned xrd0 = (unsigned)(px * 64 + (((q >> 1) ^ ((px >> 2) & 3)) * 16) + (q & 1) * 8);
  const unsigned xrd1 = (unsigned)(px * 64 + (((2 + (q >> 1)) ^ ((px >> 2) & 3)) * 16) + (q & 1) * 8);
  const unsigned xfl = lds0 + XF_OFF + wv * 2048;
  bf16x8 A2f[8], B2[4][2];
  f32x4 o2[4][2];
  u32x2 xq[4][2];
  const unsigned owbase = lds0 + (w < 3 ? R2 + w * 8192 : R1 + 8192) + (q >> 1) * HOUT_PLANE + (q & 1) * 8 + (4 * T * 16 + px) * 16;
  const unsigned hr = lds0 + q * HOUT_PLANE + (4 * T * 16 + px) * 16;
  const unsigned hr2 = hr + R2, hr1 = hr + R1 + 8192;
  auto put_hout = [&](auto rc) LAMBDA {
    constexpr int r = decltype(rc)::value;
    *LDSP(u32x2, ldsw(owbase) + r * 256) = pack4(acc[r][0]);
    *LDSP(u32x2, ldsw(owbase) + 2 * HOUT_PLANE + r * 256) = pack4(acc[r][1]);
  };
  auto b2 = [&](int ks, int i) LAMBDA -> bf16x8 { return lds_b128((ks < 3 ? ldsp(hr2) + ks * 8192 : ldsp(hr1)) + i * 256); };
  static_for<0, 12>([&](auto sc) LAMBDA {
    constexpr int s = decltype(sc)::value;
    constexpr int cur = s & 1, nxt = cur ^ 1;
    SCHED();
    if constexpr ((s & 1) == 1) BAR();
    SCHED();
    static_for<0, 24>([&](auto mc) LAMBDA {
      constexpr int m = decltype(mc)::value;
      constexpr int di = s < 11 ? m / 8 : (m % 6) / 2, i = s < 11 ? (m % 8) / 2 : m / 6, t = m & 1;
      acc[i][t] = mfma(join(Ah[cur][di][t][0], Ah[cur][di][t][1]), Bf[cur][i + di], acc[i][t]);
      if constexpr (s < 11) {
        constexpr int s1 = s + 1, dj = s1 >> 2, ks = s1 & 3;
        if constexpr (m < 12) {
          constexpr int rdi = m >> 2, rt = (m >> 1) & 1, rh = m & 1;
          Ah[nxt][rdi][rt][rh] = lds_tr(aptr(blk_off(s1, rdi), rt) + rh * 1024);
        } else if constexpr (m < 18) {
          constexpr int rr = m - 12;
          Bf[nxt][rr] = lds_b128(hb + ks * 4 * PLANE + (rr * 18 + dj) * 16);
        }
      }
      if constexpr (s == 8 && m >= 20) {
        constexpr int r = (m >> 1) & 1, tt = m & 1;
        xq[r][tt] = *LDSP(const u32x2, ldsp(xfl + (tt ? xrd1 : xrd0)) + r * 1024);
      }
      if constexpr (s == 11) {
        if constexpr (m < 8) A2f[m] = lds_tr_frag(aptr(wz2_off(m >> 1), m & 1));
        if constexpr (m >= 8 && m < 12) { constexpr int e = m - 8; o2[e >> 1][e & 1] = unpack4(xq[e >> 1][e & 1]); }
        if constexpr (m == 8) put_hout(IC<0>{});
        if constexpr (m == 14) put_hout(IC<1>{});
        if constexpr (m == 16) BAR();
        if constexpr (m >= 16) { constexpr int e = m - 16; B2[e & 3][e >> 2] = b2(e & 3, e >> 2); }
        if constexpr (m == 20) put_hout(IC<2>{});
        if constexpr (m == 17 || m == 18) {
          constexpr int r = m - 15;
          xq[r][0] = *LDSP(const u32x2, ldsp(xfl + xrd0) + (r & 1) * 1024);
          xq[r][1] = *LDSP(const u32x2, ldsp(xfl + xrd1) + (r & 1) * 1024);
        }
        if constexpr (m >= 20) { constexpr int e = m - 16; o2[e >> 1][e & 1] = unpack4(xq[e >> 1][e & 1]); }
      }
      SCHED();
    });
  });
  put_hout(IC<3>{});
  SCHED();
  BAR();
  {
    bf16x8 B3[4][2];
#pragma unroll
    for (int i = 0; i < 2; ++i)
#pragma unroll
      for (int ks = 0; ks < 4; ++ks) B3[ks][i] = b2(ks, 2 + i);
    SCHED();
    const unsigned ost = lds0 + H_OFF + wv * 4096;
    const unsigned ow0 = ost + xrd0, ow1 = ost + xrd1, ord = ost + lane * 16;
    u32x4 ov[4];
    auto put = [&](auto ic) LAMBDA {
      constexpr int i = decltype(ic)::value;
      *LDSP(u32x2, ldsw(ow0) + i * 1024) = pack4(o2[i][0]);
      *LDSP(u32x2, ldsw(ow1) + i * 1024) = pack4(o2[i][1]);
      ov[i] = *LDSP(const u32x4, ldsp(ord) + i * 1024);
    };
    auto store = [&](auto ic) LAMBDA {
      constexpr int i = decltype(ic)::value;
      __builtin_amdgcn_raw_buffer_store_b128(ov[i], r_o, xoff, xsoff + i * 32768, STORE_AUX);
    };
    static_for<0, 16>([&](auto mc) LAMBDA {
      constexpr int m = decltype(mc)::value, i = m >> 2, ks = m & 3;
      bf16x8 B;
      if constexpr (i < 2) B = B2[ks][i]; else B = B3[ks][i - 2];
      o2[i][0] = mfma(A2f[2 * ks],     B, o2[i][0]);
      o2[i][1] = mfma(A2f[2 * ks + 1], B, o2[i][1]);
      if constexpr (m == 5) put(IC<0>{});
      if constexpr (m == 8) store(IC<0>{});
      if constexpr (m == 9) put(IC<1>{});
      if constexpr (m == 12) store(IC<1>{});
      if constexpr (m == 13) put(IC<2>{});
      SCHED();
    });
    store(IC<2>{});
    put(IC<3>{});
    store(IC<3>{});
  }
}

extern "C" void kernel_launch(void* const* d_in, const int* in_sizes, int n_in,
                              void* d_out, int out_size, void* d_ws, size_t ws_size,
                              hipStream_t stream) {
  const __hip_bfloat16* xf  = (const __hip_bfloat16*)d_in[0];
  const __hip_bfloat16* x   = (const __hip_bfloat16*)d_in[1];
  const __hip_bfloat16* c   = (const __hip_bfloat16*)d_in[2];
  const __hip_bfloat16* wz1 = (const __hip_bfloat16*)d_in[3];
  const __hip_bfloat16* wz2 = (const __hip_bfloat16*)d_in[4];
  const __hip_bfloat16* wc  = (const __hip_bfloat16*)d_in[5];
  (void)hipFuncSetAttribute((const void*)controlnet_fused, hipFuncAttributeMaxDynamicSharedMemorySize, LDS_BYTES);
  controlnet_fused<<<256, NTHREADS, LDS_BYTES, stream>>>(xf, x, c, wz1, wz2, wc, (__hip_bfloat16*)d_out);
}
```

```cpp
#include <hip/hip_runtime.h>
#include <hip/hip_bf16.h>

typedef __attribute__((ext_vector_type(8))) __bf16 bf16x8;
typedef __attribute__((ext_vector_type(2))) __bf16 bf16x2;
typedef __attribute__((ext_vector_type(4))) short s16x4;
typedef __attribute__((ext_vector_type(8))) short s16x8;
typedef __attribute__((ext_vector_type(4))) float f32x4;
typedef __attribute__((ext_vector_type(2))) float f32x2;
typedef __attribute__((ext_vector_type(4))) unsigned u32x4;
typedef __attribute__((ext_vector_type(2))) unsigned u32x2;
typedef __attribute__((address_space(3))) char lds_char;
#define LDSP(T, p) ((__attribute__((address_space(3))) T*)(p))
#define DEVINL __device__ __forceinline__
#define LAMBDA __attribute__((always_inline))

constexpr int PLANE     = 3072;
constexpr int H_OFF     = 0;
constexpr int R0 = 49152, R1 = 81920, R2 = 114688;
constexpr int STATS_OFF = 147456;
constexpr int XF_OFF    = STATS_OFF;
constexpr int LDS_BYTES = STATS_OFF + 16384;
constexpr int HOUT_PLANE = 2048;
__host__ __device__ constexpr int reg_off(int a) { return a == 0 ? R0 : a == 1 ? R1 : R2; }
__host__ __device__ constexpr int blk_off(int s, int di) { return (s & 3) < 3 ? reg_off(s & 3) + di * 8192 : reg_off(di) + 24576; }
__host__ __device__ constexpr int wz2_off(int ks) { return ks < 3 ? R0 + ks * 8192 : R1; }
__host__ __device__ constexpr int hout_off(int ks) { return ks < 3 ? R2 + ks * 8192 : R1 + 8192; }
constexpr unsigned OOB  = 0x40000000u;
constexpr int TENSOR_BYTES = 2 * 128 * 128 * 128 * 2;
constexpr int NTHREADS = 576;

#define STAMP(i) do {} while (0)
#define STORE_AUX 17
#define WAIT_VM(n) asm volatile("s_waitcnt vmcnt(" #n ")" ::: "memory")
#define BAR() asm volatile("s_waitcnt lgkmcnt(0)\n\ts_barrier" ::: "memory")
#define SCHED() __builtin_amdgcn_sched_barrier(0)

template <int N> struct IC { static constexpr int value = N; };
template <int I, int N, class F> DEVINL void static_for(F&& f) { if constexpr (I < N) { f(IC<I>{}); static_for<I + 1, N>(f); } }

DEVINL u32x4 make_rsrc(const void* p, unsigned bytes) {
  unsigned long long a = (unsigned long long)p;
  u32x4 r = {(unsigned)a, (unsigned)(a >> 32) & 0xffffu, bytes, 0x00020000u};
  return r;
}
DEVINL void dma16(u32x4 rsrc, unsigned voff, unsigned soff, unsigned lds_addr) {
  asm volatile("s_mov_b32 m0, %0\n\ts_nop 0\n\tbuffer_load_dwordx4 %1, %2, %3 offen lds" :: "s"(lds_addr), "v"(voff), "s"(rsrc), "s"(soff) : "memory");
}
DEVINL void dma16_nt(u32x4 rsrc, unsigned voff, unsigned soff, unsigned lds_addr) {
  asm volatile("s_mov_b32 m0, %0\n\ts_nop 0\n\tbuffer_load_dwordx4 %1, %2, %3 offen nt lds" :: "s"(lds_addr), "v"(voff), "s"(rsrc), "s"(soff) : "memory");
}
DEVINL void dma16x4(u32x4 rsrc, unsigned v0, unsigned v1, unsigned v2, unsigned v3, unsigned soff, unsigned lds_addr) {
  asm volatile("s_mov_b32 m0, %0\n\ts_nop 0\n\t"
               "buffer_load_dwordx4 %1, %5, %6 offen lds\n\t"
               "buffer_load_dwordx4 %2, %5, %6 offen offset:1024 lds\n\t"
               "buffer_load_dwordx4 %3, %5, %6 offen offset:2048 lds\n\t"
               "buffer_load_dwordx4 %4, %5, %6 offen offset:3072 lds"
               :: "s"(lds_addr), "v"(v0), "v"(v1), "v"(v2), "v"(v3), "s"(rsrc), "s"(soff) : "memory");
}
DEVINL void dma16x4_nt(u32x4 rsrc, unsigned v0, unsigned v1, unsigned v2, unsigned v3, unsigned soff, unsigned lds_addr) {
  asm volatile("s_mov_b32 m0, %0\n\ts_nop 0\n\t"
               "buffer_load_dwordx4 %1, %5, %6 offen nt lds\n\t"
               "buffer_load_dwordx4 %2, %5, %6 offen offset:1024 nt lds\n\t"
               "buffer_load_dwordx4 %3, %5, %6 offen offset:2048 nt lds\n\t"
               "buffer_load_dwordx4 %4, %5, %6 offen offset:3072 nt lds"
               :: "s"(lds_addr), "v"(v0), "v"(v1), "v"(v2), "v"(v3), "s"(rsrc), "s"(soff) : "memory");
}
DEVINL const lds_char* ldsp(unsigned a) { return (const lds_char*)(size_t)a; }
DEVINL lds_char* ldsw(unsigned a) { return (lds_char*)(size_t)a; }
DEVINL bf16x8 lds_b128(const lds_char* p) { return *LDSP(const bf16x8, p); }
DEVINL s16x4 lds_tr(const lds_char* p) { return __builtin_amdgcn_ds_read_tr16_b64_v4i16(LDSP(s16x4, p)); }
DEVINL bf16x8 join(s16x4 lo, s16x4 hi) { return __builtin_bit_cast(bf16x8, __builtin_shufflevector(lo, hi, 0, 1, 2, 3, 4, 5, 6, 7)); }
DEVINL bf16x8 lds_tr_frag(const lds_char* p) { return join(lds_tr(p), lds_tr(p + 1024)); }
DEVINL f32x4 mfma(bf16x8 a, bf16x8 b, f32x4 c) { return __builtin_amdgcn_mfma_f32_16x16x32_bf16(a, b, c, 0, 0, 0); }
DEVINL unsigned pk2v(f32x2 f) { return __builtin_bit_cast(unsigned, __builtin_convertvector(f, bf16x2)); }
DEVINL f32x2 lo2(f32x4 v) { return __builtin_shufflevector(v, v, 0, 1); }
DEVINL f32x2 hi2(f32x4 v) { return __builtin_shufflevector(v, v, 2, 3); }
DEVINL u32x2 pack4(f32x4 v) { u32x2 r = {pk2v(lo2(v)), pk2v(hi2(v))}; return r; }
DEVINL float bf_lo(unsigned u) { return __builtin_bit_cast(float, u << 16); }
DEVINL float bf_hi(unsigned u) { return __builtin_bit_cast(float, u & 0xffff0000u); }
DEVINL unsigned f2u(float f) { return __builtin_bit_cast(unsigned, f); }
DEVINL float u2f(unsigned u) { return __builtin_bit_cast(float, u); }
DEVINL f32x4 unpack4(u32x2 v) { f32x4 r = {bf_lo(v[0]), bf_hi(v[0]), bf_lo(v[1]), bf_hi(v[1])}; return r; }
DEVINL f32x2 bc2(float v) { f32x2 r = {v, v}; return r; }
DEVINL f32x2 fma2(f32x2 a, f32x2 b, f32x2 c) { return __builtin_elementwise_fma(a, b, c); }
DEVINL float reduce4(float a, float b, float c, float d) {
  auto r0 = __builtin_amdgcn_permlane16_swap(f2u(a), f2u(b), false, false);
  auto r1 = __builtin_amdgcn_permlane16_swap(f2u(c), f2u(d), false, false);
  float ab = u2f(r0[0]) + u2f(r0[1]);
  float cd = u2f(r1[0]) + u2f(r1[1]);
  auto r2 = __builtin_amdgcn_permlane32_swap(f2u(ab), f2u(cd), false, false);
  return u2f(r2[0]) + u2f(r2[1]);
}

__global__ __launch_bounds__(NTHREADS, 3)
void controlnet_fused(const __hip_bfloat16* __restrict__ xf, const __hip_bfloat16* __restrict__ x,
                      const __hip_bfloat16* __restrict__ c, const __hip_bfloat16* __restrict__ wz1,
                      const __hip_bfloat16* __restrict__ wz2, const __hip_bfloat16* __restrict__ wconv,
                      __hip_bfloat16* __restrict__ out
                      ) {
  extern __shared__ __attribute__((aligned(16))) char smem_generic[];
  const unsigned lds0 = (unsigned)(size_t)(lds_char*)smem_generic;

  const int tid = threadIdx.x;
  const int lane = tid & 63;
  const int wv = __builtin_amdgcn_readfirstlane(tid >> 6);
  const int q = lane >> 4, px = lane & 15;
  const int bid = blockIdx.x;
  const int tile = (bid & 7) * 32 + (bid >> 3);
  const int bimg = tile >> 7, ty0 = ((tile >> 3) & 15) * 8, tx0 = (tile & 7) * 16;
  const u32x4 r_c  = make_rsrc(c, TENSOR_BYTES), r_x = make_rsrc(x, TENSOR_BYTES), r_xf = make_rsrc(xf, TENSOR_BYTES);

  auto grp_row = [&](int T, int j) LAMBDA -> int { return T == 0 ? j : (j == 0 ? 9 : j + 4); };
  auto row_soff = [&](int r) LAMBDA -> unsigned {
    const int gy = ty0 - 1 + r;
    return (unsigned)gy < 128u ? (unsigned)(((bimg * 128 + gy) * 128 + tx0) * 256) : OOB;
  };
  auto row_voff = [&](int w_) LAMBDA -> unsigned { const int p = 4 * w_ + q; return (unsigned)(p * 256 + ((px ^ p) * 16)); };
  auto col_voff = [&](int T, int w_) LAMBDA -> unsigned {
    const int p = 4 * w_ + q;
    const int gy = ty0 - 1 + 5 * T + (p >> 1), gx = tx0 - 1 + ((p & 1) ? 17 : 0);
    const bool ok = p < 10 && (unsigned)gy < 128u && (unsigned)gx < 128u;
    return ok ? (unsigned)(((bimg * 128 + gy) * 128 + gx) * 256 + ((px ^ p) * 16)) : OOB;
  };

  if (wv == 8) {
    unsigned rv[4];
#pragma unroll
    for (int w_ = 0; w_ < 4; ++w_) rv[w_] = row_voff(w_) - w_ * 1024;
    auto rows = [&](int j) LAMBDA {
#pragma unroll
      for (int T = 0; T < 2; ++T) {
        const unsigned so = row_soff(grp_row(T, j));
        const unsigned dst = lds0 + reg_off(j >> 1) + (T * 2 + (j & 1)) * 8192;
        dma16x4_nt(r_c, rv[0], rv[1], rv[2], rv[3], so, dst);
        dma16x4_nt(r_x, rv[0], rv[1], rv[2], rv[3], so, dst + 4096);
      }
    };
    unsigned tw[4], twx;
    {
      const u32x4 r_w2t = make_rsrc(wz2, 32768), r_wct = make_rsrc(wconv, 9 * 32768);
      const unsigned sl = (unsigned)(bid >> 3);
      const unsigned o0 = sl * 9216 + lane * 128;
      const unsigned o1 = lane < 8 ? sl * 9216 + (64 + lane) * 128 : OOB;
      const unsigned o2 = lane < 8 ? sl * 1024 + lane * 128 : OOB;
      asm volatile("buffer_load_dword %0, %1, %2, 0 offen" : "=v"(tw[0]) : "v"(o0), "s"(r_wct) : "memory");
      asm volatile("buffer_load_dword %0, %1, %2, 0 offen" : "=v"(tw[1]) : "v"(o1), "s"(r_wct) : "memory");
      asm volatile("buffer_load_dword %0, %1, %2, 0 offen" : "=v"(tw[2]) : "v"(o2), "s"(r_w2t) : "memory");
      const u32x4 r_ot = make_rsrc(out, TENSOR_BYTES);
      const unsigned o3 = lane == 0 ? (unsigned)(((bimg * 128 + ty0) * 128 + tx0) * 256) : OOB;
      asm volatile("buffer_load_dword %0, %1, %2, 0 offen" : "=v"(tw[3]) : "v"(lane < 32 ? o3 : OOB), "s"(r_ot) : "memory");
      unsigned tw4;
      asm volatile("buffer_load_dword %0, %1, %2, 0 offen nt" : "=v"(tw4) : "v"(o3), "s"(r_xf) : "memory");
      twx = tw4;
    }
    BAR();
    rows(2); rows(3);
    BAR();
    rows(4);
#pragma unroll
    for (int T = 0; T < 2; ++T) {
      unsigned cv[4];
#pragma unroll
      for (int w_ = 0; w_ < 4; ++w_) { unsigned o = col_voff(T, w_); cv[w_] = o == OOB ? OOB : o - w_ * 1024; }
      const unsigned dst = lds0 + R2 + (T * 2 + 1) * 8192;
      dma16x4_nt(r_c, cv[0], cv[1], cv[2], cv[3], 0, dst);
      dma16x4_nt(r_x, cv[0], cv[1], cv[2], cv[3], 0, dst + 4096);
    }
    const u32x4 r_w2 = make_rsrc(wz2, 32768), r_wc = make_rsrc(wconv, 9 * 32768);
    const unsigned vA = (unsigned)(q * 256 + ((px ^ (q << 1)) * 16));
    const unsigned vB = (unsigned)(q * 256 + ((px ^ ((q | 4) << 1)) * 16));
    auto block = [&](u32x4 rs, int src, int dst) LAMBDA {
      dma16x4(rs, vA, vA, vB, vB, src, lds0 + dst);
      dma16x4(rs, vA, vA, vB, vB, src + 4096, lds0 + dst + 4096);
    };
    auto stage = [&](int s) LAMBDA {
#pragma unroll
      for (int di = 0; di < 3; ++di) block(r_wc, (di * 3 + (s >> 2)) * 32768 + (s & 3) * 8192, blk_off(s, di));
    };
    WAIT_VM(32); BAR();
    stage(0);
    WAIT_VM(24); BAR();
    stage(1);
    WAIT_VM(24); BAR();
    stage(2); stage(3);
    WAIT_VM(48); BAR();
    stage(4);
    WAIT_VM(24); BAR();
    stage(5);
    WAIT_VM(0); BAR();
    const unsigned xlv = (unsigned)((lane >> 2) * 256 + (((lane & 3) ^ ((lane >> 4) & 3)) * 16));
    auto xf_rows = [&](int r0) LAMBDA {
#pragma unroll
      for (int v = 0; v < 8; ++v)
#pragma unroll
        for (int r = 0; r < 2; ++r)
          dma16_nt(r_xf, xlv, (unsigned)(((bimg * 128 + ty0 + 4 * (v >> 2) + r0 + r) * 128 + tx0) * 256 + (v & 3) * 64), lds0 + XF_OFF + v * 2048 + r * 1024);
    };
    stage(6); stage(7);
    xf_rows(0);
    WAIT_VM(16); BAR();
    stage(8); stage(9);
    WAIT_VM(0); BAR();
    stage(10); stage(11);
    WAIT_VM(0); BAR();
    block(r_w2, 0, wz2_off(0)); block(r_w2, 8192, wz2_off(1)); block(r_w2, 16384, wz2_off(2)); block(r_w2, 24576, wz2_off(3));
    xf_rows(2);
    WAIT_VM(16); BAR();
    WAIT_VM(0); BAR();
    asm volatile("" :: "v"(tw[0]), "v"(tw[1]), "v"(tw[2]), "v"(tw[3]), "v"(twx));
    return;
  }

  const int T = wv >> 2, w = wv & 3;
  STAMP(0);
  {
    const u32x4 r_w1 = make_rsrc(wz1, 32768);
    const int wk = 4 * wv + q;
    const int wf = (wk & 3) | (((wk >> 3) & 1) << 2);
    const unsigned wvoff = (unsigned)(wk * 256 + ((px ^ (wf << 1)) * 16));
#pragma unroll
    for (int ks = 0; ks < 4; ++ks) dma16(r_w1, wvoff, ks * 8192, lds0 + wv * 1024 + H_OFF + ks * 8192);
    const unsigned rv = row_voff(w);
#pragma unroll
    for (int j = 0; j < 2; ++j) {
      const unsigned so = row_soff(grp_row(T, j));
      const unsigned dst = lds0 + R0 + (T * 2 + j) * 8192 + w * 1024;
      dma16_nt(r_c, rv, so, dst);
      dma16_nt(r_x, rv, so, dst + 4096);
    }
  }
  __amdgpu_buffer_rsrc_t r_o = __builtin_amdgcn_make_buffer_rsrc((void*)out, 0, TENSOR_BYTES, 0x00020000);
  const int aq = (lane & 15) >> 2, ap = lane & 3;
  const int af = aq | ((q & 1) << 2);
  const unsigned ab0 = (unsigned)((8 * q + aq) * 256 + (((2 * w) ^ af) * 32) + 8 * ap);
  const unsigned aH0 = lds0 + ab0, aH1 = lds0 + (ab0 ^ 32);
  const unsigned aR0 = aH0 + R0, aR1 = aH1 + R0;
  const unsigned aS0 = aH0 + R2, aS1 = aH1 + R2;
  auto aptr = [&](int off, int t) LAMBDA -> const lds_char* {
    if (off < R0) return ldsp(t ? aH1 : aH0) + off;
    if (off < R2) return ldsp(t ? aR1 : aR0) + (off - R0);
    return ldsp(t ? aS1 : aS0) + (off - R2);
  };
  unsigned hw[6];
  {
    const unsigned hwbase = lds0 + H_OFF + (4 * w + (q >> 1)) * PLANE + (q & 1) * 8;
#pragma unroll
    for (int j = 0; j < 5; ++j) hw[j] = hwbase + (grp_row(T, j) * 18 + px + 1) * 16;
    hw[5] = hwbase + (px < 10 ? (5 * T + (px >> 1)) * 18 + ((px & 1) ? 17 : 0) : 180 + 6 * T + (px - 10)) * 16;
  }
  unsigned cb[4], cb2[4];
#pragma unroll
  for (int ks = 0; ks < 4; ++ks) { cb[ks] = lds0 + R0 + T * 16384 + px * 256 + (((4 * ks + q) ^ px) * 16); cb2[ks] = cb[ks] + 65536; }
  const unsigned xb0 = lds0 + R0 + T * 16384 + 4096 + px * 256 + (((4 * w + (q >> 1)) ^ px) * 16) + (q & 1) * 8;
  const unsigned xb1 = lds0 + R0 + T * 16384 + 4096 + px * 256 + (((4 * w + 2 + (q >> 1)) ^ px) * 16) + (q & 1) * 8;
  const unsigned xb2 = xb0 + 65536, xb3 = xb1 + 65536;
  const unsigned stw = lds0 + STATS_OFF + (w >> 1) * 3072 + ((T * 6 + (q >> 1)) * 16 + px) * 16 + (w & 1) * 8 + (q & 1) * 4;
  const unsigned strh = lds0 + STATS_OFF + ((T * 6 + (q >> 1)) * 16 + px) * 16;

  f32x4 hin[6][2];
  bf16x8 A1f[4][2];
  bf16x8 Cb[2][4]; u32x2 Xb[2][2];
  auto z1_read = [&](auto kc) LAMBDA {
    constexpr int k = decltype(kc)::value;
#pragma unroll
    for (int jj = 0; jj < 2; ++jj) {
      Xb[jj][0] = *LDSP(const u32x2, ldsp(k < 2 ? xb0 : xb2) + (k & 1) * 32768 + jj * 8192);
      Xb[jj][1] = *LDSP(const u32x2, ldsp(k < 2 ? xb1 : xb3) + (k & 1) * 32768 + jj * 8192);
    }
#pragma unroll
    for (int ks = 0; ks < 4; ++ks)
#pragma unroll
      for (int jj = 0; jj < 2; ++jj) Cb[jj][ks] = lds_b128(ldsp(k < 2 ? cb[ks] : cb2[ks]) + (k & 1) * 32768 + jj * 8192);
  };
  auto z1_unpack = [&](auto kc) LAMBDA {
    constexpr int k = decltype(kc)::value;
#pragma unroll
    for (int jj = 0; jj < 2; ++jj) {
      hin[2 * k + jj][0] = unpack4(Xb[jj][0]);
      hin[2 * k + jj][1] = unpack4(Xb[jj][1]);
    }
  };
  auto z1_mma1 = [&](auto kc, auto ic) LAMBDA {
    constexpr int k = decltype(kc)::value, i = decltype(ic)::value;
    constexpr int ks = i >> 2, jj = (i >> 1) & 1, t = i & 1;
    hin[2 * k + jj][t] = mfma(A1f[ks][t], Cb[jj][ks], hin[2 * k + jj][t]);
  };
  auto z1_stats = [&](auto kc) LAMBDA {
    constexpr int k = decltype(kc)::value;
    float st[4];
#pragma unroll
    for (int jj = 0; jj < 2; ++jj) {
      const int j = 2 * k + jj;
      f32x2 a = (lo2(hin[j][0]) + hi2(hin[j][0])) + (lo2(hin[j][1]) + hi2(hin[j][1]));
      f32x2 b = lo2(hin[j][0]) * lo2(hin[j][0]);
      b = fma2(hi2(hin[j][0]), hi2(hin[j][0]), b);
      b = fma2(lo2(hin[j][1]), lo2(hin[j][1]), b);
      b = fma2(hi2(hin[j][1]), hi2(hin[j][1]), b);
      st[2 * jj] = a[0] + a[1];
      st[2 * jj + 1] = b[0] + b[1];
    }
    float r = reduce4(st[0], st[1], st[2], st[3]);
    *LDSP(float, ldsw(stw) + 2 * k * 256) = r;
  };
  f32x4 sp0, sp1;
  auto si_read = [&](auto kc) LAMBDA {
    constexpr int k = decltype(kc)::value;
    sp0 = *LDSP(const f32x4, ldsp(strh) + 2 * k * 256);
    sp1 = *LDSP(const f32x4, ldsp(strh) + 2 * k * 256 + 3072);
  };
  auto si_math = [&](auto kc, auto hook) LAMBDA {
    constexpr int k = decltype(kc)::value;
    float rs2[2], nb2[2];
    {
      f32x2 m = ((lo2(sp0) + hi2(sp0)) + (lo2(sp1) + hi2(sp1))) * bc2(1.f / 128.f);
      float var = __builtin_fmaxf(__builtin_fmaf(-m[0], m[0], m[1]), m[1] * 1e-6f);
      float r = __builtin_amdgcn_rsqf(var + 1e-5f) * -1.4426950408889634f;
      float n = -m[0] * r;
      auto a = __builtin_amdgcn_permlane32_swap(f2u(r), f2u(r), false, false);
      auto b = __builtin_amdgcn_permlane32_swap(f2u(n), f2u(n), false, false);
      rs2[0] = u2f(a[0]); rs2[1] = u2f(a[1]); nb2[0] = u2f(b[0]); nb2[1] = u2f(b[1]);
    }
    f32x2 t[8], e[8];
#pragma unroll
    for (int g = 0; g < 4; ++g) {
      t[2 * g]     = fma2(lo2(hin[2 * k + (g >> 1)][g & 1]), bc2(rs2[g >> 1]), bc2(nb2[g >> 1]));
      t[2 * g + 1] = fma2(hi2(hin[2 * k + (g >> 1)][g & 1]), bc2(rs2[g >> 1]), bc2(nb2[g >> 1]));
    }
    SCHED();
    static_for<0, 16>([&](auto ic) LAMBDA {
      constexpr int i = decltype(ic)::value;
      hook(ic);
      e[i >> 1][i & 1] = __builtin_amdgcn_exp2f(t[i >> 1][i & 1]);
      SCHED();
    });
    f32x2 P[4]; float rr[4];
#pragma unroll
    for (int g = 0; g < 4; ++g) { f32x2 g1 = e[2 * g] + bc2(1.f); P[g] = fma2(g1, e[2 * g + 1], g1); }
#pragma unroll
    for (int g = 0; g < 4; ++g) rr[g] = __builtin_amdgcn_rcpf(P[g][0] * P[g][1]);
#pragma unroll
    for (int g = 0; g < 4; ++g) { t[2 * g] = fma2(t[2 * g], e[2 * g + 1], t[2 * g]); t[2 * g + 1] = fma2(t[2 * g + 1], e[2 * g], t[2 * g + 1]); }
#pragma unroll
    for (int g = 0; g < 4; ++g) {
      f32x2 sw = __builtin_shufflevector(P[g], P[g], 1, 0);
      P[g] = sw * bc2(rr[g] * -0.6931471805599453f);
    }
#pragma unroll
    for (int g = 0; g < 4; ++g) {
      u32x2 pk = {pk2v(t[2 * g] * P[g]), pk2v(t[2 * g + 1] * P[g])};
      *LDSP(u32x2, ldsw(hw[2 * k + (g >> 1)]) + 2 * (g & 1) * PLANE) = pk;
    }
  };

  asm volatile("" :: "v"(aH0), "v"(aH1), "v"(aR0), "v"(aR1), "v"(aS0), "v"(aS1), "v"(hw[0]), "v"(hw[1]), "v"(hw[2]), "v"(hw[3]), "v"(hw[4]), "v"(hw[5]));
  asm volatile("" :: "v"(cb[0]), "v"(cb[1]), "v"(cb[2]), "v"(cb[3]), "v"(cb2[0]), "v"(cb2[1]), "v"(cb2[2]), "v"(cb2[3]), "v"(xb0), "v"(xb1), "v"(xb2), "v"(xb3), "v"(stw), "v"(strh));
  WAIT_VM(4);
  BAR();
#pragma unroll
  for (int ks = 0; ks < 4; ++ks) {
    A1f[ks][0] = lds_tr_frag(aptr(H_OFF + ks * 8192, 0));
    A1f[ks][1] = lds_tr_frag(aptr(H_OFF + ks * 8192, 1));
  }
  WAIT_VM(0);
  BAR();
  STAMP(1);
  z1_read(IC<0>{});
  SCHED();
  z1_unpack(IC<0>{});
  static_for<0, 16>([&](auto ic) LAMBDA { z1_mma1(IC<0>{}, ic); });
  z1_stats(IC<0>{});
  SCHED();
  BAR();
  STAMP(3);
  si_read(IC<0>{});
  z1_read(IC<1>{});
  SCHED();
  z1_unpack(IC<1>{});
  si_math(IC<0>{}, [&](auto ic) LAMBDA { z1_mma1(IC<1>{}, ic); });
  z1_stats(IC<1>{});
  SCHED();
  BAR();
  STAMP(5);
  si_read(IC<1>{});
  z1_read(IC<2>{});
  SCHED();
  z1_unpack(IC<2>{});
  si_math(IC<1>{}, [&](auto ic) LAMBDA { z1_mma1(IC<2>{}, ic); });
  z1_stats(IC<2>{});
  SCHED();
  BAR();
  STAMP(7);
  si_read(IC<2>{});
  s16x4 Ah[2][3][2][2];
#pragma unroll
  for (int di = 0; di < 3; ++di)
#pragma unroll
    for (int t = 0; t < 2; ++t) {
      Ah[0][di][t][0] = lds_tr(aptr(blk_off(0, di), t));
      Ah[0][di][t][1] = lds_tr(aptr(blk_off(0, di), t) + 1024);
    }
  SCHED();
  si_math(IC<2>{}, [&](auto) LAMBDA {});
  f32x4 acc[4][2];
#pragma unroll
  for (int i = 0; i < 4; ++i) { acc[i][0] = hin[i + 1][0]; acc[i][1] = hin[i + 1][1]; }
  SCHED();
  BAR();
  STAMP(9);

  const lds_char* hb = ldsp(lds0 + H_OFF + q * PLANE + (T * 4 * 18 + px) * 16);
  bf16x8 Bf[2][6];
#pragma unroll
  for (int rr = 0; rr < 6; ++rr) Bf[0][rr] = lds_b128(hb + rr * 18 * 16);
  const int lp = lane >> 2, lc = lane & 3;
  const unsigned xoff = (unsigned)(lp * 256 + (4 * w + (lc ^ ((lp >> 2) & 3))) * 16);
  const unsigned xsoff = (unsigned)(((bimg * 128 + ty0 + 4 * T) * 128 + tx0) * 256);
  const unsigned xrd0 = (unsigned)(px * 64 + (((q >> 1) ^ ((px >> 2) & 3)) * 16) + (q & 1) * 8);
  const unsigned xrd1 = (unsigned)(px * 64 + (((2 + (q >> 1)) ^ ((px >> 2) & 3)) * 16) + (q & 1) * 8);
  const unsigned xfl = lds0 + XF_OFF + wv * 2048;
  bf16x8 A2f[8], B2[4][2];
  f32x4 o2[4][2];
  u32x2 xq[4][2];
  const unsigned owbase = lds0 + (w < 3 ? R2 + w * 8192 : R1 + 8192) + (q >> 1) * HOUT_PLANE + (q & 1) * 8 + (4 * T * 16 + px) * 16;
  const unsigned hr = lds0 + q * HOUT_PLANE + (4 * T * 16 + px) * 16;
  const unsigned hr2 = hr + R2, hr1 = hr + R1 + 8192;
  auto put_hout = [&](auto rc) LAMBDA {
    constexpr int r = decltype(rc)::value;
    *LDSP(u32x2, ldsw(owbase) + r * 256) = pack4(acc[r][0]);
    *LDSP(u32x2, ldsw(owbase) + 2 * HOUT_PLANE + r * 256) = pack4(acc[r][1]);
  };
  auto b2 = [&](int ks, int i) LAMBDA -> bf16x8 { return lds_b128((ks < 3 ? ldsp(hr2) + ks * 8192 : ldsp(hr1)) + i * 256); };
  static_for<0, 12>([&](auto sc) LAMBDA {
    constexpr int s = decltype(sc)::value;
    constexpr int cur = s & 1, nxt = cur ^ 1;
    SCHED();
    if constexpr ((s & 1) == 1) BAR();
    SCHED();
    static_for<0, 24>([&](auto mc) LAMBDA {
      constexpr int m = decltype(mc)::value;
      constexpr int di = s < 11 ? m / 8 : (m % 6) / 2, i = s < 11 ? (m % 8) / 2 : m / 6, t = m & 1;
      acc[i][t] = mfma(join(Ah[cur][di][t][0], Ah[cur][di][t][1]), Bf[cur][i + di], acc[i][t]);
      if constexpr (s < 11) {
        constexpr int s1 = s + 1, dj = s1 >> 2, ks = s1 & 3;
        if constexpr (m < 12) {
          constexpr int rdi = m >> 2, rt = (m >> 1) & 1, rh = m & 1;
          Ah[nxt][rdi][rt][rh] = lds_tr(aptr(blk_off(s1, rdi), rt) + rh * 1024);
        } else if constexpr (m < 18) {
          constexpr int rr = m - 12;
          Bf[nxt][rr] = lds_b128(hb + ks * 4 * PLANE + (rr * 18 + dj) * 16);
        }
      }
      if constexpr (s == 8 && m >= 20) {
        constexpr int r = (m >> 1) & 1, tt = m & 1;
        xq[r][tt] = *LDSP(const u32x2, ldsp(xfl + (tt ? xrd1 : xrd0)) + r * 1024);
      }
      if constexpr (s == 11) {
        if constexpr (m < 8) A2f[m] = lds_tr_frag(aptr(wz2_off(m >> 1), m & 1));
        if constexpr (m >= 8 && m < 12) { constexpr int e = m - 8; o2[e >> 1][e & 1] = unpack4(xq[e >> 1][e & 1]); }
        if constexpr (m == 8) put_hout(IC<0>{});
        if constexpr (m == 14) put_hout(IC<1>{});
        if constexpr (m == 16) BAR();
        if constexpr (m >= 16) { constexpr int e = m - 16; B2[e & 3][e >> 2] = b2(e & 3, e >> 2); }
        if constexpr (m == 20) put_hout(IC<2>{});
        if constexpr (m == 17 || m == 18) {
          constexpr int r = m - 15;
          xq[r][0] = *LDSP(const u32x2, ldsp(xfl + xrd0) + (r & 1) * 1024);
          xq[r][1] = *LDSP(const u32x2, ldsp(xfl + xrd1) + (r & 1) * 1024);
        }
        if constexpr (m >= 20) { constexpr int e = m - 16; o2[e >> 1][e & 1] = unpack4(xq[e >> 1][e & 1]); }
      }
      SCHED();
    });
  });
  put_hout(IC<3>{});
  SCHED();
  STAMP(10);
  BAR();
  STAMP(11);
  {
    bf16x8 B3[4][2];
#pragma unroll
    for (int i = 0; i < 2; ++i)
#pragma unroll
      for (int ks = 0; ks < 4; ++ks) B3[ks][i] = b2(ks, 2 + i);
    SCHED();
    const unsigned ost = lds0 + H_OFF + wv * 4096;
    const unsigned ow0 = ost + xrd0, ow1 = ost + xrd1, ord = ost + lane * 16;
    u32x4 ov[4];
    auto put = [&](auto ic) LAMBDA {
      constexpr int i = decltype(ic)::value;
      *LDSP(u32x2, ldsw(ow0) + i * 1024) = pack4(o2[i][0]);
      *LDSP(u32x2, ldsw(ow1) + i * 1024) = pack4(o2[i][1]);
      ov[i] = *LDSP(const u32x4, ldsp(ord) + i * 1024);
    };
    auto store = [&](auto ic) LAMBDA {
      constexpr int i = decltype(ic)::value;
      __builtin_amdgcn_raw_buffer_store_b128(ov[i], r_o, xoff, xsoff + i * 32768, STORE_AUX);
    };
    static_for<0, 16>([&](auto mc) LAMBDA {
      constexpr int m = decltype(mc)::value, i = m >> 2, ks = m & 3;
      bf16x8 B;
      if constexpr (i < 2) B = B2[ks][i]; else B = B3[ks][i - 2];
      o2[i][0] = mfma(A2f[2 * ks],     B, o2[i][0]);
      o2[i][1] = mfma(A2f[2 * ks + 1], B, o2[i][1]);
      if constexpr (m == 5) put(IC<0>{});
      if constexpr (m == 8) store(IC<0>{});
      if constexpr (m == 9) put(IC<1>{});
      if constexpr (m == 12) store(IC<1>{});
      if constexpr (m == 13) put(IC<2>{});
      SCHED();
    });
    store(IC<2>{});
    put(IC<3>{});
    store(IC<3>{});
  }
  STAMP(12);
}

extern "C" void kernel_launch(void* const* d_in, const int* in_sizes, int n_in,
                              void* d_out, int out_size, void* d_ws, size_t ws_size,
                              hipStream_t stream) {
  const __hip_bfloat16* xf  = (const __hip_bfloat16*)d_in[0];
  const __hip_bfloat16* x   = (const __hip_bfloat16*)d_in[1];
  const __hip_bfloat16* c   = (const __hip_bfloat16*)d_in[2];
  const __hip_bfloat16* wz1 = (const __hip_bfloat16*)d_in[3];
  const __hip_bfloat16* wz2 = (const __hip_bfloat16*)d_in[4];
  const __hip_bfloat16* wc  = (const __hip_bfloat16*)d_in[5];
  (void)hipFuncSetAttribute((const void*)controlnet_fused, hipFuncAttributeMaxDynamicSharedMemorySize, LDS_BYTES);
  controlnet_fused<<<256, NTHREADS, LDS_BYTES, stream>>>(xf, x, c, wz1, wz2, wc, (__hip_bfloat16*)d_out
  );
}
```

```cpp
#include <hip/hip_runtime.h>
#include <hip/hip_bf16.h>

typedef __attribute__((ext_vector_type(8))) __bf16 bf16x8;
typedef __attribute__((ext_vector_type(2))) __bf16 bf16x2;
typedef __attribute__((ext_vector_type(4))) short s16x4;
typedef __attribute__((ext_vector_type(8))) short s16x8;
typedef __attribute__((ext_vector_type(4))) float f32x4;
typedef __attribute__((ext_vector_type(2))) float f32x2;
typedef __attribute__((ext_vector_type(4))) unsigned u32x4;
typedef __attribute__((ext_vector_type(2))) unsigned u32x2;
typedef __attribute__((address_space(3))) char lds_char;
#define LDSP(T, p) ((__attribute__((address_space(3))) T*)(p))
#define DEVINL __device__ __forceinline__
#define LAMBDA __attribute__((always_inline))

constexpr int PLANE     = 3072;
constexpr int H_OFF     = 0;
constexpr int R0 = 49152, R1 = 81920, R2 = 114688;
constexpr int STATS_OFF = 147456;
constexpr int XF_OFF    = STATS_OFF;
constexpr int LDS_BYTES = STATS_OFF + 16384;
constexpr int HOUT_PLANE = 2048;
__host__ __device__ constexpr int reg_off(int a) { return a == 0 ? R0 : a == 1 ? R1 : R2; }
__host__ __device__ constexpr int blk_off(int s, int di) { return (s & 3) < 3 ? reg_off(s & 3) + di * 8192 : reg_off(di) + 24576; }
__host__ __device__ constexpr int wz2_off(int ks) { return ks < 3 ? R0 + ks * 8192 : R1; }
__host__ __device__ constexpr int hout_off(int ks) { return ks < 3 ? R2 + ks * 8192 : R1 + 8192; }
constexpr unsigned OOB  = 0x40000000u;
constexpr int TENSOR_BYTES = 2 * 128 * 128 * 128 * 2;
constexpr int NTHREADS = 576;

constexpr int STORE_AUX = 17;
#define WAIT_VM(n) asm volatile("s_waitcnt vmcnt(" #n ")" ::: "memory")
#define BAR() asm volatile("s_waitcnt lgkmcnt(0)\n\ts_barrier" ::: "memory")
#define SCHED() __builtin_amdgcn_sched_barrier(0)

template <int N> struct IC { static constexpr int value = N; };
template <int I, int N, class F> DEVINL void static_for(F&& f) { if constexpr (I < N) { f(IC<I>{}); static_for<I + 1, N>(f); } }

DEVINL u32x4 make_rsrc(const void* p, unsigned bytes) {
  unsigned long long a = (unsigned long long)p;
  u32x4 r = {(unsigned)a, (unsigned)(a >> 32) & 0xffffu, bytes, 0x00020000u};
  return r;
}
DEVINL void dma16(u32x4 rsrc, unsigned voff, unsigned soff, unsigned lds_addr) {
  asm volatile("s_mov_b32 m0, %0\n\ts_nop 0\n\tbuffer_load_dwordx4 %1, %2, %3 offen lds" :: "s"(lds_addr), "v"(voff), "s"(rsrc), "s"(soff) : "memory");
}
DEVINL void dma16_nt(u32x4 rsrc, unsigned voff, unsigned soff, unsigned lds_addr) {
  asm volatile("s_mov_b32 m0, %0\n\ts_nop 0\n\tbuffer_load_dwordx4 %1, %2, %3 offen nt lds" :: "s"(lds_addr), "v"(voff), "s"(rsrc), "s"(soff) : "memory");
}
DEVINL void dma16x4(u32x4 rsrc, unsigned v0, unsigned v1, unsigned v2, unsigned v3, unsigned soff, unsigned lds_addr) {
  asm volatile("s_mov_b32 m0, %0\n\ts_nop 0\n\t"
               "buffer_load_dwordx4 %1, %5, %6 offen lds\n\t"
               "buffer_load_dwordx4 %2, %5, %6 offen offset:1024 lds\n\t"
               "buffer_load_dwordx4 %3, %5, %6 offen offset:2048 lds\n\t"
               "buffer_load_dwordx4 %4, %5, %6 offen offset:3072 lds"
               :: "s"(lds_addr), "v"(v0), "v"(v1), "v"(v2), "v"(v3), "s"(rsrc), "s"(soff) : "memory");
}
DEVINL void dma16x4_nt(u32x4 rsrc, unsigned v0, unsigned v1, unsigned v2, unsigned v3, unsigned soff, unsigned lds_addr) {
  asm volatile("s_mov_b32 m0, %0\n\ts_nop 0\n\t"
               "buffer_load_dwordx4 %1, %5, %6 offen nt lds\n\t"
               "buffer_load_dwordx4 %2, %5, %6 offen offset:1024 nt lds\n\t"
               "buffer_load_dwordx4 %3, %5, %6 offen offset:2048 nt lds\n\t"
               "buffer_load_dwordx4 %4, %5, %6 offen offset:3072 nt lds"
               :: "s"(lds_addr), "v"(v0), "v"(v1), "v"(v2), "v"(v3), "s"(rsrc), "s"(soff) : "memory");
}
DEVINL const lds_char* ldsp(unsigned a) { return (const lds_char*)(size_t)a; }
DEVINL lds_char* ldsw(unsigned a) { return (lds_char*)(size_t)a; }
DEVINL bf16x8 lds_b128(const lds_char* p) { return *LDSP(const bf16x8, p); }
DEVINL s16x4 lds_tr(const lds_char* p) { return __builtin_amdgcn_ds_read_tr16_b64_v4i16(LDSP(s16x4, p)); }
DEVINL bf16x8 join(s16x4 lo, s16x4 hi) { return __builtin_bit_cast(bf16x8, __builtin_shufflevector(lo, hi, 0, 1, 2, 3, 4, 5, 6, 7)); }
DEVINL bf16x8 lds_tr_frag(const lds_char* p) { return join(lds_tr(p), lds_tr(p + 1024)); }
DEVINL f32x4 mfma(bf16x8 a, bf16x8 b, f32x4 c) { return __builtin_amdgcn_mfma_f32_16x16x32_bf16(a, b, c, 0, 0, 0); }
DEVINL unsigned pk2v(f32x2 f) { return __builtin_bit_cast(unsigned, __builtin_convertvector(f, bf16x2)); }
DEVINL f32x2 lo2(f32x4 v) { return __builtin_shufflevector(v, v, 0, 1); }
DEVINL f32x2 hi2(f32x4 v) { return __builtin_shufflevector(v, v, 2, 3); }
DEVINL u32x2 pack4(f32x4 v) { u32x2 r = {pk2v(lo2(v)), pk2v(hi2(v))}; return r; }
DEVINL float bf_lo(unsigned u) { return __builtin_bit_cast(float, u << 16); }
DEVINL float bf_hi(unsigned u) { return __builtin_bit_cast(float, u & 0xffff0000u); }
DEVINL unsigned f2u(float f) { return __builtin_bit_cast(unsigned, f); }
DEVINL float u2f(unsigned u) { return __builtin_bit_cast(float, u); }
DEVINL f32x4 unpack4(u32x2 v) { f32x4 r = {bf_lo(v[0]), bf_hi(v[0]), bf_lo(v[1]), bf_hi(v[1])}; return r; }
DEVINL f32x2 bc2(float v) { f32x2 r = {v, v}; return r; }
DEVINL f32x2 fma2(f32x2 a, f32x2 b, f32x2 c) { return __builtin_elementwise_fma(a, b, c); }
DEVINL float reduce4(float a, float b, float c, float d) {
  auto r0 = __builtin_amdgcn_permlane16_swap(f2u(a), f2u(b), false, false);
  auto r1 = __builtin_amdgcn_permlane16_swap(f2u(c), f2u(d), false, false);
  float ab = u2f(r0[0]) + u2f(r0[1]);
  float cd = u2f(r1[0]) + u2f(r1[1]);
  auto r2 = __builtin_amdgcn_permlane32_swap(f2u(ab), f2u(cd), false, false);
  return u2f(r2[0]) + u2f(r2[1]);
}

__global__ __launch_bounds__(NTHREADS, 3)
void controlnet_fused(const __hip_bfloat16* __restrict__ xf, const __hip_bfloat16* __restrict__ x,
                      const __hip_bfloat16* __restrict__ c, const __hip_bfloat16* __restrict__ wz1,
                      const __hip_bfloat16* __restrict__ wz2, const __hip_bfloat16* __restrict__ wconv,
                      __hip_bfloat16* __restrict__ out) {
  extern __shared__ __attribute__((aligned(16))) char smem_generic[];
  const unsigned lds0 = (unsigned)(size_t)(lds_char*)smem_generic;

  const int tid = threadIdx.x;
  const int lane = tid & 63;
  const int wv = __builtin_amdgcn_readfirstlane(tid >> 6);
  const int q = lane >> 4, px = lane & 15;
  const int bid = blockIdx.x;
  const int tile = (bid & 7) * 32 + (bid >> 3);
  const int bimg = tile >> 7, ty0 = ((tile >> 3) & 15) * 8, tx0 = (tile & 7) * 16;
  const u32x4 r_c  = make_rsrc(c, TENSOR_BYTES), r_x = make_rsrc(x, TENSOR_BYTES), r_xf = make_rsrc(xf, TENSOR_BYTES);

  auto grp_row = [&](int T, int j) LAMBDA -> int { return T == 0 ? j : (j == 0 ? 9 : j + 4); };
  auto row_soff = [&](int r) LAMBDA -> unsigned {
    const int gy = ty0 - 1 + r;
    return (unsigned)gy < 128u ? (unsigned)(((bimg * 128 + gy) * 128 + tx0) * 256) : OOB;
  };
  auto row_voff = [&](int w_) LAMBDA -> unsigned { const int p = 4 * w_ + q; return (unsigned)(p * 256 + ((px ^ p) * 16)); };
  auto col_voff = [&](int T, int w_) LAMBDA -> unsigned {
    const int p = 4 * w_ + q;
    const int gy = ty0 - 1 + 5 * T + (p >> 1), gx = tx0 - 1 + ((p & 1) ? 17 : 0);
    const bool ok = p < 10 && (unsigned)gy < 128u && (unsigned)gx < 128u;
    return ok ? (unsigned)(((bimg * 128 + gy) * 128 + gx) * 256 + ((px ^ p) * 16)) : OOB;
  };

  if (wv == 8) {
    unsigned rv[4];
#pragma unroll
    for (int w_ = 0; w_ < 4; ++w_) rv[w_] = row_voff(w_) - w_ * 1024;
    auto rows = [&](int j) LAMBDA {
#pragma unroll
      for (int T = 0; T < 2; ++T) {
        const unsigned so = row_soff(grp_row(T, j));
        const unsigned dst = lds0 + reg_off(j >> 1) + (T * 2 + (j & 1)) * 8192;
        dma16x4_nt(r_c, rv[0], rv[1], rv[2], rv[3], so, dst);
        dma16x4_nt(r_x, rv[0], rv[1], rv[2], rv[3], so, dst + 4096);
      }
    };
    unsigned tw[5];
    {
      const u32x4 r_w2t = make_rsrc(wz2, 32768), r_wct = make_rsrc(wconv, 9 * 32768), r_ot = make_rsrc(out, TENSOR_BYTES);
      const unsigned sl = (unsigned)(bid >> 3);
      const unsigned o0 = sl * 9216 + lane * 128;
      const unsigned o1 = lane < 8 ? sl * 9216 + (64 + lane) * 128 : OOB;
      const unsigned o2 = lane < 8 ? sl * 1024 + lane * 128 : OOB;
      const unsigned o3 = lane == 0 ? (unsigned)(((bimg * 128 + ty0) * 128 + tx0) * 256) : OOB;
      asm volatile("buffer_load_dword %0, %1, %2, 0 offen" : "=v"(tw[0]) : "v"(o0), "s"(r_wct) : "memory");
      asm volatile("buffer_load_dword %0, %1, %2, 0 offen" : "=v"(tw[1]) : "v"(o1), "s"(r_wct) : "memory");
      asm volatile("buffer_load_dword %0, %1, %2, 0 offen" : "=v"(tw[2]) : "v"(o2), "s"(r_w2t) : "memory");
      asm volatile("buffer_load_dword %0, %1, %2, 0 offen" : "=v"(tw[3]) : "v"(o3), "s"(r_ot) : "memory");
      asm volatile("buffer_load_dword %0, %1, %2, 0 offen nt" : "=v"(tw[4]) : "v"(o3), "s"(r_xf) : "memory");
    }
    BAR();
    rows(2); rows(3);
    BAR();
    rows(4);
#pragma unroll
    for (int T = 0; T < 2; ++T) {
      unsigned cv[4];
#pragma unroll
      for (int w_ = 0; w_ < 4; ++w_) { unsigned o = col_voff(T, w_); cv[w_] = o == OOB ? OOB : o - w_ * 1024; }
      const unsigned dst = lds0 + R2 + (T * 2 + 1) * 8192;
      dma16x4_nt(r_c, cv[0], cv[1], cv[2], cv[3], 0, dst);
      dma16x4_nt(r_x, cv[0], cv[1], cv[2], cv[3], 0, dst + 4096);
    }
    const u32x4 r_w2 = make_rsrc(wz2, 32768), r_wc = make_rsrc(wconv, 9 * 32768);
    const unsigned vA = (unsigned)(q * 256 + ((px ^ (q << 1)) * 16));
    const unsigned vB = (unsigned)(q * 256 + ((px ^ ((q | 4) << 1)) * 16));
    auto block = [&](u32x4 rs, int src, int dst) LAMBDA {
      dma16x4(rs, vA, vA, vB, vB, src, lds0 + dst);
      dma16x4(rs, vA, vA, vB, vB, src + 4096, lds0 + dst + 4096);
    };
    auto stage = [&](int s) LAMBDA {
#pragma unroll
      for (int di = 0; di < 3; ++di) block(r_wc, (di * 3 + (s >> 2)) * 32768 + (s & 3) * 8192, blk_off(s, di));
    };
    WAIT_VM(32); BAR();
    stage(0);
    WAIT_VM(24); BAR();
    stage(1);
    WAIT_VM(24); BAR();
    stage(2); stage(3);
    WAIT_VM(48); BAR();
    stage(4);
    WAIT_VM(24); BAR();
    stage(5);
    WAIT_VM(0); BAR();
    const unsigned xlv = (unsigned)((lane >> 2) * 256 + (((lane & 3) ^ ((lane >> 4) & 3)) * 16));
    auto xf_rows = [&](int r0) LAMBDA {
#pragma unroll
      for (int v = 0; v < 8; ++v)
#pragma unroll
        for (int r = 0; r < 2; ++r)
          dma16_nt(r_xf, xlv, (unsigned)(((bimg * 128 + ty0 + 4 * (v >> 2) + r0 + r) * 128 + tx0) * 256 + (v & 3) * 64), lds0 + XF_OFF + v * 2048 + r * 1024);
    };
    stage(6); stage(7);
    xf_rows(0);
    WAIT_VM(16); BAR();
    stage(8); stage(9);
    WAIT_VM(0); BAR();
    stage(10); stage(11);
    WAIT_VM(0); BAR();
    block(r_w2, 0, wz2_off(0)); block(r_w2, 8192, wz2_off(1)); block(r_w2, 16384, wz2_off(2)); block(r_w2, 24576, wz2_off(3));
    xf_rows(2);
    WAIT_VM(16); BAR();
    WAIT_VM(0); BAR();
    asm volatile("" :: "v"(tw[0]), "v"(tw[1]), "v"(tw[2]), "v"(tw[3]), "v"(tw[4]));
    return;
  }

  const int T = wv >> 2, w = wv & 3;
  {
    const u32x4 r_w1 = make_rsrc(wz1, 32768);
    const int wk = 4 * wv + q;
    const int wf = (wk & 3) | (((wk >> 3) & 1) << 2);
    const unsigned wvoff = (unsigned)(wk * 256 + ((px ^ (wf << 1)) * 16));
#pragma unroll
    for (int ks = 0; ks < 4; ++ks) dma16(r_w1, wvoff, ks * 8192, lds0 + wv * 1024 + H_OFF + ks * 8192);
    const unsigned rv = row_voff(w);
#pragma unroll
    for (int j = 0; j < 2; ++j) {
      const unsigned so = row_soff(grp_row(T, j));
      const unsigned dst = lds0 + R0 + (T * 2 + j) * 8192 + w * 1024;
      dma16_nt(r_c, rv, so, dst);
      dma16_nt(r_x, rv, so, dst + 4096);
    }
  }
  __amdgpu_buffer_rsrc_t r_o = __builtin_amdgcn_make_buffer_rsrc((void*)out, 0, TENSOR_BYTES, 0x00020000);
  const int aq = (lane & 15) >> 2, ap = lane & 3;
  const int af = aq | ((q & 1) << 2);
  const unsigned ab0 = (unsigned)((8 * q + aq) * 256 + (((2 * w) ^ af) * 32) + 8 * ap);
  const unsigned aH0 = lds0 + ab0, aH1 = lds0 + (ab0 ^ 32);
  const unsigned aR0 = aH0 + R0, aR1 = aH1 + R0;
  const unsigned aS0 = aH0 + R2, aS1 = aH1 + R2;
  auto aptr = [&](int off, int t) LAMBDA -> const lds_char* {
    if (off < R0) return ldsp(t ? aH1 : aH0) + off;
    if (off < R2) return ldsp(t ? aR1 : aR0) + (off - R0);
    return ldsp(t ? aS1 : aS0) + (off - R2);
  };
  unsigned hw[6];
  {
    const unsigned hwbase = lds0 + H_OFF + (4 * w + (q >> 1)) * PLANE + (q & 1) * 8;
#pragma unroll
    for (int j = 0; j < 5; ++j) hw[j] = hwbase + (grp_row(T, j) * 18 + px + 1) * 16;
    hw[5] = hwbase + (px < 10 ? (5 * T + (px >> 1)) * 18 + ((px & 1) ? 17 : 0) : 180 + 6 * T + (px - 10)) * 16;
  }
  unsigned cb[4], cb2[4];
#pragma unroll
  for (int ks = 0; ks < 4; ++ks) { cb[ks] = lds0 + R0 + T * 16384 + px * 256 + (((4 * ks + q) ^ px) * 16); cb2[ks] = cb[ks] + 65536; }
  const unsigned xb0 = lds0 + R0 + T * 16384 + 4096 + px * 256 + (((4 * w + (q >> 1)) ^ px) * 16) + (q & 1) * 8;
  const unsigned xb1 = lds0 + R0 + T * 16384 + 4096 + px * 256 + (((4 * w + 2 + (q >> 1)) ^ px) * 16) + (q & 1) * 8;
  const unsigned xb2 = xb0 + 65536, xb3 = xb1 + 65536;
  const unsigned stw = lds0 + STATS_OFF + (w >> 1) * 3072 + ((T * 6 + (q >> 1)) * 16 + px) * 16 + (w & 1) * 8 + (q & 1) * 4;
  const unsigned strh = lds0 + STATS_OFF + ((T * 6 + (q >> 1)) * 16 + px) * 16;

  f32x4 hin[6][2];
  bf16x8 A1f[4][2];
  bf16x8 Cb[2][4]; u32x2 Xb[2][2];
  auto z1_read = [&](auto kc) LAMBDA {
    constexpr int k = decltype(kc)::value;
#pragma unroll
    for (int jj = 0; jj < 2; ++jj) {
      Xb[jj][0] = *LDSP(const u32x2, ldsp(k < 2 ? xb0 : xb2) + (k & 1) * 32768 + jj * 8192);
      Xb[jj][1] = *LDSP(const u32x2, ldsp(k < 2 ? xb1 : xb3) + (k & 1) * 32768 + jj * 8192);
    }
#pragma unroll
    for (int ks = 0; ks < 4; ++ks)
#pragma unroll
      for (int jj = 0; jj < 2; ++jj) Cb[jj][ks] = lds_b128(ldsp(k < 2 ? cb[ks] : cb2[ks]) + (k & 1) * 32768 + jj * 8192);
  };
  auto z1_unpack = [&](auto kc) LAMBDA {
    constexpr int k = decltype(kc)::value;
#pragma unroll
    for (int jj = 0; jj < 2; ++jj) {
      hin[2 * k + jj][0] = unpack4(Xb[jj][0]);
      hin[2 * k + jj][1] = unpack4(Xb[jj][1]);
    }
  };
  auto z1_mma1 = [&](auto kc, auto ic) LAMBDA {
    constexpr int k = decltype(kc)::value, i = decltype(ic)::value;
    constexpr int ks = i >> 2, jj = (i >> 1) & 1, t = i & 1;
    hin[2 * k + jj][t] = mfma(A1f[ks][t], Cb[jj][ks], hin[2 * k + jj][t]);
  };
  auto z1_stats = [&](auto kc) LAMBDA {
    constexpr int k = decltype(kc)::value;
    float st[4];
#pragma unroll
    for (int jj = 0; jj < 2; ++jj) {
      const int j = 2 * k + jj;
      f32x2 a = (lo2(hin[j][0]) + hi2(hin[j][0])) + (lo2(hin[j][1]) + hi2(hin[j][1]));
      f32x2 b = lo2(hin[j][0]) * lo2(hin[j][0]);
      b = fma2(hi2(hin[j][0]), hi2(hin[j][0]), b);
      b = fma2(lo2(hin[j][1]), lo2(hin[j][1]), b);
      b = fma2(hi2(hin[j][1]), hi2(hin[j][1]), b);
      st[2 * jj] = a[0] + a[1];
      st[2 * jj + 1] = b[0] + b[1];
    }
    float r = reduce4(st[0], st[1], st[2], st[3]);
    *LDSP(float, ldsw(stw) + 2 * k * 256) = r;
  };
  f32x4 sp0, sp1;
  auto si_read = [&](auto kc) LAMBDA {
    constexpr int k = decltype(kc)::value;
    sp0 = *LDSP(const f32x4, ldsp(strh) + 2 * k * 256);
    sp1 = *LDSP(const f32x4, ldsp(strh) + 2 * k * 256 + 3072);
  };
  auto si_math = [&](auto kc, auto hook) LAMBDA {
    constexpr int k = decltype(kc)::value;
    float rs2[2], nb2[2];
    {
      f32x2 m = ((lo2(sp0) + hi2(sp0)) + (lo2(sp1) + hi2(sp1))) * bc2(1.f / 128.f);
      float var = __builtin_fmaxf(__builtin_fmaf(-m[0], m[0], m[1]), m[1] * 1e-6f);
      float r = __builtin_amdgcn_rsqf(var + 1e-5f) * -1.4426950408889634f;
      float n = -m[0] * r;
      auto a = __builtin_amdgcn_permlane32_swap(f2u(r), f2u(r), false, false);
      auto b = __builtin_amdgcn_permlane32_swap(f2u(n), f2u(n), false, false);
      rs2[0] = u2f(a[0]); rs2[1] = u2f(a[1]); nb2[0] = u2f(b[0]); nb2[1] = u2f(b[1]);
    }
    f32x2 t[8], e[8];
#pragma unroll
    for (int g = 0; g < 4; ++g) {
      t[2 * g]     = fma2(lo2(hin[2 * k + (g >> 1)][g & 1]), bc2(rs2[g >> 1]), bc2(nb2[g >> 1]));
      t[2 * g + 1] = fma2(hi2(hin[2 * k + (g >> 1)][g & 1]), bc2(rs2[g >> 1]), bc2(nb2[g >> 1]));
    }
    SCHED();
    static_for<0, 16>([&](auto ic) LAMBDA {
      constexpr int i = decltype(ic)::value;
      hook(ic);
      e[i >> 1][i & 1] = __builtin_amdgcn_exp2f(t[i >> 1][i & 1]);
      SCHED();
    });
    f32x2 P[4]; float rr[4];
#pragma unroll
    for (int g = 0; g < 4; ++g) { f32x2 g1 = e[2 * g] + bc2(1.f); P[g] = fma2(g1, e[2 * g + 1], g1); }
#pragma unroll
    for (int g = 0; g < 4; ++g) rr[g] = __builtin_amdgcn_rcpf(P[g][0] * P[g][1]);
#pragma unroll
    for (int g = 0; g < 4; ++g) { t[2 * g] = fma2(t[2 * g], e[2 * g + 1], t[2 * g]); t[2 * g + 1] = fma2(t[2 * g + 1], e[2 * g], t[2 * g + 1]); }
#pragma unroll
    for (int g = 0; g < 4; ++g) {
      f32x2 sw = __builtin_shufflevector(P[g], P[g], 1, 0);
      P[g] = sw * bc2(rr[g] * -0.6931471805599453f);
    }
#pragma unroll
    for (int g = 0; g < 4; ++g) {
      u32x2 pk = {pk2v(t[2 * g] * P[g]), pk2v(t[2 * g + 1] * P[g])};
      *LDSP(u32x2, ldsw(hw[2 * k + (g >> 1)]) + 2 * (g & 1) * PLANE) = pk;
    }
  };

  asm volatile("" :: "v"(aH0), "v"(aH1), "v"(aR0), "v"(aR1), "v"(aS0), "v"(aS1), "v"(hw[0]), "v"(hw[1]), "v"(hw[2]), "v"(hw[3]), "v"(hw[4]), "v"(hw[5]));
  asm volatile("" :: "v"(cb[0]), "v"(cb[1]), "v"(cb[2]), "v"(cb[3]), "v"(cb2[0]), "v"(cb2[1]), "v"(cb2[2]), "v"(cb2[3]), "v"(xb0), "v"(xb1), "v"(xb2), "v"(xb3), "v"(stw), "v"(strh));
  WAIT_VM(4);
  BAR();
#pragma unroll
  for (int ks = 0; ks < 4; ++ks) {
    A1f[ks][0] = lds_tr_frag(aptr(H_OFF + ks * 8192, 0));
    A1f[ks][1] = lds_tr_frag(aptr(H_OFF + ks * 8192, 1));
  }
  WAIT_VM(0);
  BAR();
  z1_read(IC<0>{});
  SCHED();
  z1_unpack(IC<0>{});
  static_for<0, 16>([&](auto ic) LAMBDA { z1_mma1(IC<0>{}, ic); });
  z1_stats(IC<0>{});
  SCHED();
  BAR();
  si_read(IC<0>{});
  z1_read(IC<1>{});
  SCHED();
  z1_unpack(IC<1>{});
  si_math(IC<0>{}, [&](auto ic) LAMBDA { z1_mma1(IC<1>{}, ic); });
  z1_stats(IC<1>{});
  SCHED();
  BAR();
  si_read(IC<1>{});
  z1_read(IC<2>{});
  SCHED();
  z1_unpack(IC<2>{});
  si_math(IC<1>{}, [&](auto ic) LAMBDA { z1_mma1(IC<2>{}, ic); });
  z1_stats(IC<2>{});
  SCHED();
  BAR();
  si_read(IC<2>{});
  s16x4 Ah[2][3][2][2];
#pragma unroll
  for (int di = 0; di < 3; ++di)
#pragma unroll
    for (int t = 0; t < 2; ++t) {
      Ah[0][di][t][0] = lds_tr(aptr(blk_off(0, di), t));
      Ah[0][di][t][1] = lds_tr(aptr(blk_off(0, di), t) + 1024);
    }
  SCHED();
  si_math(IC<2>{}, [&](auto) LAMBDA {});
  f32x4 acc[4][2];
#pragma unroll
  for (int i = 0; i < 4; ++i) { acc[i][0] = hin[i + 1][0]; acc[i][1] = hin[i + 1][1]; }
  SCHED();
  BAR();

  const lds_char* hb = ldsp(lds0 + H_OFF + q * PLANE + (T * 4 * 18 + px) * 16);
  bf16x8 Bf[2][6];
#pragma unroll
  for (int rr = 0; rr < 6; ++rr) Bf[0][rr] = lds_b128(hb + rr * 18 * 16);
  const int lp = lane >> 2, lc = lane & 3;
  const unsigned xoff = (unsigned)(lp * 256 + (4 * w + (lc ^ ((lp >> 2) & 3))) * 16);
  const unsigned xsoff = (unsigned)(((bimg * 128 + ty0 + 4 * T) * 128 + tx0) * 256);
  const unsigned xrd0 = (unsigned)(px * 64 + (((q >> 1) ^ ((px >> 2) & 3)) * 16) + (q & 1) * 8);
  const unsigned xrd1 = (unsigned)(px * 64 + (((2 + (q >> 1)) ^ ((px >> 2) & 3)) * 16) + (q & 1) * 8);
  const unsigned xfl = lds0 + XF_OFF + wv * 2048;
  bf16x8 A2f[8], B2[4][2];
  f32x4 o2[4][2];
  u32x2 xq[4][2];
  const unsigned owbase = lds0 + (w < 3 ? R2 + w * 8192 : R1 + 8192) + (q >> 1) * HOUT_PLANE + (q & 1) * 8 + (4 * T * 16 + px) * 16;
  const unsigned hr = lds0 + q * HOUT_PLANE + (4 * T * 16 + px) * 16;
  const unsigned hr2 = hr + R2, hr1 = hr + R1 + 8192;
  auto put_hout = [&](auto rc) LAMBDA {
    constexpr int r = decltype(rc)::value;
    *LDSP(u32x2, ldsw(owbase) + r * 256) = pack4(acc[r][0]);
    *LDSP(u32x2, ldsw(owbase) + 2 * HOUT_PLANE + r * 256) = pack4(acc[r][1]);
  };
  auto b2 = [&](int ks, int i) LAMBDA -> bf16x8 { return lds_b128((ks < 3 ? ldsp(hr2) + ks * 8192 : ldsp(hr1)) + i * 256); };
  static_for<0, 12>([&](auto sc) LAMBDA {
    constexpr int s = decltype(sc)::value;
    constexpr int cur = s & 1, nxt = cur ^ 1;
    SCHED();
    if constexpr ((s & 1) == 1) BAR();
    SCHED();
    static_for<0, 24>([&](auto mc) LAMBDA {
      constexpr int m = decltype(mc)::value;
      constexpr int di = s < 11 ? m / 8 : (m % 6) / 2, i = s < 11 ? (m % 8) / 2 : m / 6, t = m & 1;
      acc[i][t] = mfma(join(Ah[cur][di][t][0], Ah[cur][di][t][1]), Bf[cur][i + di], acc[i][t]);
      if constexpr (s < 11) {
        constexpr int s1 = s + 1, dj = s1 >> 2, ks = s1 & 3;
        if constexpr (m < 12) {
          constexpr int rdi = m >> 2, rt = (m >> 1) & 1, rh = m & 1;
          Ah[nxt][rdi][rt][rh] = lds_tr(aptr(blk_off(s1, rdi), rt) + rh * 1024);
        } else if constexpr (m < 18) {
          constexpr int rr = m - 12;
          Bf[nxt][rr] = lds_b128(hb + ks * 4 * PLANE + (rr * 18 + dj) * 16);
        }
      }
      if constexpr (s == 8 && m >= 20) {
        constexpr int r = (m >> 1) & 1, tt = m & 1;
        xq[r][tt] = *LDSP(const u32x2, ldsp(xfl + (tt ? xrd1 : xrd0)) + r * 1024);
      }
      if constexpr (s == 11) {
        if constexpr (m < 8) A2f[m] = lds_tr_frag(aptr(wz2_off(m >> 1), m & 1));
        if constexpr (m >= 8 && m < 12) { constexpr int e = m - 8; o2[e >> 1][e & 1] = unpack4(xq[e >> 1][e & 1]); }
        if constexpr (m == 8) put_hout(IC<0>{});
        if constexpr (m == 14) put_hout(IC<1>{});
        if constexpr (m == 16) BAR();
        if constexpr (m >= 16) { constexpr int e = m - 16; B2[e & 3][e >> 2] = b2(e & 3, e >> 2); }
        if constexpr (m == 20) put_hout(IC<2>{});
        if constexpr (m == 17 || m == 18) {
          constexpr int r = m - 15;
          xq[r][0] = *LDSP(const u32x2, ldsp(xfl + xrd0) + (r & 1) * 1024);
          xq[r][1] = *LDSP(const u32x2, ldsp(xfl + xrd1) + (r & 1) * 1024);
        }
        if constexpr (m >= 20) { constexpr int e = m - 16; o2[e >> 1][e & 1] = unpack4(xq[e >> 1][e & 1]); }
      }
      SCHED();
    });
  });
  put_hout(IC<3>{});
  SCHED();
  BAR();
  {
    bf16x8 B3[4][2];
#pragma unroll
    for (int i = 0; i < 2; ++i)
#pragma unroll
      for (int ks = 0; ks < 4; ++ks) B3[ks][i] = b2(ks, 2 + i);
    SCHED();
    const unsigned ost = lds0 + H_OFF + wv * 4096;
    const unsigned ow0 = ost + xrd0, ow1 = ost + xrd1, ord = ost + lane * 16;
    u32x4 ov[4];
    auto put = [&](auto ic) LAMBDA {
      constexpr int i = decltype(ic)::value;
      *LDSP(u32x2, ldsw(ow0) + i * 1024) = pack4(o2[i][0]);
      *LDSP(u32x2, ldsw(ow1) + i * 1024) = pack4(o2[i][1]);
      ov[i] = *LDSP(const u32x4, ldsp(ord) + i * 1024);
    };
    auto store = [&](auto ic) LAMBDA {
      constexpr int i = decltype(ic)::value;
      __builtin_amdgcn_raw_buffer_store_b128(ov[i], r_o, xoff, xsoff + i * 32768, STORE_AUX);
    };
    static_for<0, 16>([&](auto mc) LAMBDA {
      constexpr int m = decltype(mc)::value, i = m >> 2, ks = m & 3;
      bf16x8 B;
      if constexpr (i < 2) B = B2[ks][i]; else B = B3[ks][i - 2];
      o2[i][0] = mfma(A2f[2 * ks],     B, o2[i][0]);
      o2[i][1] = mfma(A2f[2 * ks + 1], B, o2[i][1]);
      if constexpr (m == 5) put(IC<0>{});
      if constexpr (m == 8) store(IC<0>{});
      if constexpr (m == 9) put(IC<1>{});
      if constexpr (m == 12) store(IC<1>{});
      if constexpr (m == 13) put(IC<2>{});
      SCHED();
    });
    store(IC<2>{});
    put(IC<3>{});
    store(IC<3>{});
  }
}

extern "C" void kernel_launch(void* const* d_in, const int* in_sizes, int n_in,
                              void* d_out, int out_size, void* d_ws, size_t ws_size,
                              hipStream_t stream) {
  const __hip_bfloat16* xf  = (const __hip_bfloat16*)d_in[0];
  const __hip_bfloat16* x   = (const __hip_bfloat16*)d_in[1];
  const __hip_bfloat16* c   = (const __hip_bfloat16*)d_in[2];
  const __hip_bfloat16* wz1 = (const __hip_bfloat16*)d_in[3];
  const __hip_bfloat16* wz2 = (const __hip_bfloat16*)d_in[4];
  const __hip_bfloat16* wc  = (const __hip_bfloat16*)d_in[5];
  (void)hipFuncSetAttribute((const void*)controlnet_fused, hipFuncAttributeMaxDynamicSharedMemorySize, LDS_BYTES);
  controlnet_fused<<<256, NTHREADS, LDS_BYTES, stream>>>(xf, x, c, wz1, wz2, wc, (__hip_bfloat16*)d_out);
}
```
